```python
import torch

def _softmax(x, axis=-1):
    return torch.softmax(x, dim=axis)
def _gelu(x):
    return torch.nn.functional.gelu(x, approximate="tanh")
def _silu(x):
    return torch.nn.functional.silu(x)
def _sigmoid(x):
    return torch.sigmoid(x)
def _layernorm(x, axis=-1, eps=1e-5):
    m = x.mean(dim=axis, keepdim=True)
    v = x.var(dim=axis, keepdim=True, unbiased=False)
    return (x - m) / torch.sqrt(v + eps)
def _scatter_add(tgt, idx, src, axis=0):
    return tgt.clone().index_add_(axis, idx, src)
def _segment_sum(src, seg, n):
    out = torch.zeros((n,) + src.shape[1:], dtype=src.dtype)
    return out.index_add_(0, seg, src)
def _pad(x, pw):
    flat = tuple(v for pair in reversed(pw) for v in pair)
    return torch.nn.functional.pad(x, flat)

kernel_name = 'concat_heads__sliding_local_dilated__softmax_full_mha__prompt'

def setup_inputs():

    _w = (lambda a: (lambda t: t.to(torch.bfloat16) if t.is_floating_point() else t)(torch.as_tensor(a)))
    def _A_setup():
        B, S, D = 1, 2048, 512
        return {
            "x": _w((lambda *s: torch.randn(*s, dtype=torch.bfloat16) * 0.4)(B, S, D)),
            "wqkv": _w((lambda *s: torch.randn(*s, dtype=torch.bfloat16) * 0.4)(D, 3 * D)),
            "wo": _w((lambda *s: torch.randn(*s, dtype=torch.bfloat16) * 0.05)(D, D)),
        }
    _A_in = _A_setup()

    def _B_setup():
        B, S, D = 1, 2048, 512
        return {
            "x": _w((lambda *s: torch.randn(*s, dtype=torch.bfloat16) * 0.2)(B, S, D)),
            "wqkv": _w((lambda *s: torch.randn(*s, dtype=torch.bfloat16) * 0.4)(D, 3 * D)),
            "wo": _w((lambda *s: torch.randn(*s, dtype=torch.bfloat16) * 0.1)(D, D)),
        }
    _B_in = _B_setup()

    return {
        "A_x": _A_in["x"],
        "A_wqkv": _A_in["wqkv"],
        "A_wo": _A_in["wo"],
        "B_x": _B_in["x"],
        "B_wqkv": _B_in["wqkv"],
        "B_wo": _B_in["wo"],
    }

def reference(A_x, A_wqkv, A_wo, B_x, B_wqkv, B_wo):
    def _A_ref(x, wqkv, wo):
        B, S, D = x.shape
        H = 32; Dh = D // H
        W, dil = 8, 2
        qkv = (lambda x, *d: x.permute(*d))((x @ wqkv).reshape(B, S, 3, H, Dh), 2, 0, 3, 1, 4)
        q, k, v = qkv[0], qkv[1], qkv[2]
        scale = Dh ** -0.5
        pos = torch.arange(S)
        off = torch.arange(W) * dil
        idx = pos[:, None] - off[None, :]
        mask = idx >= 0
        idx_c = torch.where(mask, idx, 0).reshape(-1)
        kg = k[:, :, idx_c, :].reshape(B, H, S, W, Dh)
        vg = v[:, :, idx_c, :].reshape(B, H, S, W, Dh)
        logits = torch.einsum("bhsd,bhswd->bhsw", q, kg) * scale
        logits = torch.where(mask[None, None, :, :], logits, -1e9)
        attn = _softmax(logits, axis=-1)
        o = torch.einsum("bhsw,bhswd->bhsd", attn, vg)
        return (lambda x, *d: x.permute(*d))(o, 0, 2, 1, 3).reshape(B, S, D) @ wo

    def _B_ref(x, wqkv, wo):
        B, S, D = x.shape
        H = 32; Dh = D // H
        qkv = (lambda x, *d: x.permute(*d))((x @ wqkv).reshape(B, S, 3, H, Dh), 2, 0, 3, 1, 4)
        q, k, v = qkv[0], qkv[1], qkv[2]
        scale = Dh ** -0.5
        scores = torch.einsum("bhqd,bhkd->bhqk", q, k) * scale
        p = _softmax(scores, axis=-1)
        o = torch.einsum("bhqk,bhkd->bhqd", p, v)
        return (lambda x, *d: x.permute(*d))(o, 0, 2, 1, 3).reshape(B, S, D) @ wo

    a = _A_ref(A_x, A_wqkv, A_wo)
    b = _B_ref(B_x, B_wqkv, B_wo)
    return torch.concatenate([a, b], axis=-1)
```

```cpp
#include <hip/hip_runtime.h>
#include <hip/hip_bf16.h>
#include <stdint.h>

typedef short bf16x8 __attribute__((ext_vector_type(8)));
typedef short s16x4 __attribute__((ext_vector_type(4)));
typedef float f32x4 __attribute__((ext_vector_type(4)));
typedef float f32x16 __attribute__((ext_vector_type(16)));
typedef unsigned u32x4 __attribute__((ext_vector_type(4)));
typedef unsigned u32x2 __attribute__((ext_vector_type(2)));
typedef __bf16 bf2_t __attribute__((ext_vector_type(2)));
typedef unsigned short u16;
#define LDSP(T) __attribute__((address_space(3))) T*
typedef LDSP(char) lds_ptr;

constexpr int SEQ = 2048, DM = 512, NH = 32;
constexpr float QSCALE = 0.25f * 1.4426950408889634f;
constexpr size_t WS_Q = 0, WS_K = 2u << 20, WS_V = 4u << 20, WS_OA = 6u << 20, WS_OB = 8u << 20;

#define STAMP_DECL
#define STAMP(i)
#define STAMP_OUT(k)
#define STAMP_ARG
#define STAMP_PASS
#define MFMA16(a, b, c) __builtin_amdgcn_mfma_f32_16x16x32_bf16(a, b, c, 0, 0, 0)
#define MFMA32(a, b, c) __builtin_amdgcn_mfma_f32_32x32x16_bf16(a, b, c, 0, 0, 0)
#define WAIT_BAR(N) asm volatile("s_waitcnt vmcnt(" #N ") lgkmcnt(0)\n\ts_barrier" ::: "memory")

__device__ __forceinline__ unsigned cvtpk(float lo, float hi) {
    typedef float f32x2_t __attribute__((ext_vector_type(2)));
    return __builtin_bit_cast(unsigned, __builtin_convertvector((f32x2_t){lo, hi}, bf2_t)); }
__device__ __forceinline__ void glds16(const void* g, unsigned lds_base) {
    unsigned sv; asm volatile("s_mov_b32 %0, m0\n\ts_mov_b32 m0, %2\n\ts_nop 0\n\tglobal_load_lds_dwordx4 %1, off\n\ts_mov_b32 m0, %0"
                              : "=&s"(sv) : "v"(g), "s"(lds_base) : "memory"); }
__device__ __forceinline__ void glds4(const void* g, unsigned lds_base) {
    unsigned sv; asm volatile("s_mov_b32 %0, m0\n\ts_mov_b32 m0, %2\n\ts_nop 0\n\tglobal_load_lds_dword %1, off\n\ts_mov_b32 m0, %0"
                              : "=&s"(sv) : "v"(g), "s"(lds_base) : "memory"); }
typedef short v4i16_t __attribute__((ext_vector_type(4)));
__device__ __forceinline__ s16x4 trread(lds_ptr p) {
    return __builtin_bit_cast(s16x4, __builtin_amdgcn_ds_read_tr16_b64_v4i16((LDSP(v4i16_t))p)); }
__device__ __forceinline__ bf16x8 trfrag(lds_ptr p, int second) {
    s16x4 a = trread(p), b = trread(p + second);
    return (bf16x8){a[0], a[1], a[2], a[3], b[0], b[1], b[2], b[3]}; }
__device__ __forceinline__ bf16x8 ldsfrag(lds_ptr p) { return *(LDSP(const bf16x8))p; }
__device__ __forceinline__ unsigned rfl(unsigned x) { return __builtin_amdgcn_readfirstlane(x); }

__device__ __forceinline__ u32x4 pack_pair(f32x4 a, f32x4 b, float s) {
    unsigned a0 = cvtpk(a[0] * s, a[1] * s), a1 = cvtpk(a[2] * s, a[3] * s), b0 = cvtpk(b[0] * s, b[1] * s), b1 = cvtpk(b[2] * s, b[3] * s);
    auto r0 = __builtin_amdgcn_permlane32_swap(a0, b0, false, false);
    auto r1 = __builtin_amdgcn_permlane32_swap(a1, b1, false, false);
    return (u32x4){r0[0], r1[0], r0[1], r1[1]};
}

template <bool IS_A>
__device__ __forceinline__ void qkv_phase(const u16* __restrict__ x, const u16* __restrict__ W, char* ws, int rb, int cg, char* ldsg STAMP_ARG) {
    constexpr int STAGE = IS_A ? 34816 : 32768, OFF_W = 8192, OFF_H = 32768;
    const int tid = threadIdx.x, lane = tid & 63; const int wid = rfl(tid >> 6);
    const int mi = wid >> 2, hh = wid & 3, g = lane >> 4, r16 = lane & 15;
    const unsigned lds0 = (unsigned)(uintptr_t)ldsg; lds_ptr lds = (lds_ptr)ldsg;
    const u16* xs = x + (size_t)(rb * 64 + wid * 8 + (lane >> 3)) * DM + (((lane & 7) ^ ((4 * wid + (lane >> 4)) & 7)) * 8);
    const u16* wsrc = W + (size_t)(wid * 8 + (lane >> 3)) * 1536 + cg * 64 + (((lane & 7) ^ ((((lane >> 4) & 1) | ((wid & 1) << 1)) << 1)) * 8);
    int hrow = rb * 64 - 16 + 2 * wid + (lane >> 5); hrow = hrow < 0 ? 0 : hrow;
    const u16* hs = x + (size_t)hrow * DM + ((((lane & 31) >> 2) ^ wid) * 8) + (lane & 3) * 2;
#define QKV_ISSUE(s) do { const unsigned d_ = lds0 + ((s) & 3) * STAGE + wid * 1024; \
        glds16(xs + (s) * 64, rfl(d_)); \
        glds16(wsrc + (size_t)(s) * 64 * 1536, rfl(d_ + OFF_W)); \
        glds16(wsrc + (size_t)(s) * 64 * 1536 + 512, rfl(d_ + OFF_W + 8192)); \
        glds16(wsrc + (size_t)(s) * 64 * 1536 + 1024, rfl(d_ + OFF_W + 16384)); \
        if (IS_A) glds4(hs + (s) * 64, rfl(lds0 + ((s) & 3) * STAGE + OFF_H + wid * 256)); } while (0)
    QKV_ISSUE(0); QKV_ISSUE(1); QKV_ISSUE(2);
    const int xo = r16 * 128 + ((g ^ (r16 >> 1)) * 16);
    const int phi = ((r16 >> 3) & 1) | ((g & 1) << 1);
    const int wo_ = (8 * g + (r16 >> 2)) * 128 + ((hh ^ phi) * 32) + (lane & 3) * 8;
    f32x4 aq[2] = {}, ak[2] = {}, av[2] = {}, ah = {};
#pragma unroll
    for (int s = 0; s < 8; ++s) {
        if (IS_A) { if (s <= 5) WAIT_BAR(10); else if (s == 6) WAIT_BAR(5); else WAIT_BAR(0); }
        else      { if (s <= 5) WAIT_BAR(8);  else if (s == 6) WAIT_BAR(4); else WAIT_BAR(0); }
        STAMP((IS_A ? 12 : 1) + s);
        if (s + 3 < 8) QKV_ISSUE(s + 3);
        lds_ptr st = lds + (s & 3) * STAGE;
#pragma unroll
        for (int ks = 0; ks < 2; ++ks) {
            bf16x8 x0 = ldsfrag(st + mi * 4096 + (xo ^ (ks * 64))), x1 = ldsfrag(st + mi * 4096 + 2048 + (xo ^ (ks * 64)));
            bf16x8 wq = trfrag(st + OFF_W + ks * 4096 + wo_, 512);
            bf16x8 wk = trfrag(st + OFF_W + 8192 + ks * 4096 + wo_, 512);
            bf16x8 wv = trfrag(st + OFF_W + 16384 + ks * 4096 + wo_, 512);
            aq[0] = MFMA16(wq, x0, aq[0]); aq[1] = MFMA16(wq, x1, aq[1]);
            ak[0] = MFMA16(wk, x0, ak[0]); ak[1] = MFMA16(wk, x1, ak[1]);
            if (IS_A) {
                av[0] = MFMA16(wv, x0, av[0]); av[1] = MFMA16(wv, x1, av[1]);
                bf16x8 xh = ldsfrag(st + OFF_H + (xo ^ (ks * 64)));
                ah = MFMA16(mi ? wv : wk, xh, ah);
            } else {
                av[0] = MFMA16(x0, wv, av[0]); av[1] = MFMA16(x1, wv, av[1]);
            }
        }
    }
#undef QKV_ISSUE
    STAMP(IS_A ? 20 : 9);
    const int head = cg * 4 + hh, tile = rb * 2 + mi;
    if (!IS_A) {
        const size_t fo = ((size_t)head * 64 + tile) * 1024;
        const int qo = ((lane >> 4) & 1) * 512 + (r16 + 16 * (lane >> 5)) * 16;
        *(u32x4*)(ws + WS_Q + fo + qo) = pack_pair(aq[0], aq[1], QSCALE);
        *(u32x4*)(ws + WS_K + fo + qo) = pack_pair(ak[0], ak[1], 1.0f);
        *(u32x4*)(ws + WS_V + fo + lane * 16) = pack_pair(av[0], av[1], 1.0f);
    } else {
        WAIT_BAR(0);
        constexpr int PL = 80 * 16;
        auto put = [&](int type, int row, f32x4 a, float sc) {
            u32x2 v = {cvtpk(a[0] * sc, a[1] * sc), cvtpk(a[2] * sc, a[3] * sc)};
            *(LDSP(u32x2))(lds + ((type * 4 + hh) * 2 + (g & 1)) * PL + row * 16 + (g >> 1) * 8) = v; };
        put(0, 16 + 32 * mi + r16, aq[0], QSCALE); put(0, 32 + 32 * mi + r16, aq[1], QSCALE);
        put(1, 16 + 32 * mi + r16, ak[0], 1.0f);   put(1, 32 + 32 * mi + r16, ak[1], 1.0f);
        put(2, 16 + 32 * mi + r16, av[0], 1.0f);   put(2, 32 + 32 * mi + r16, av[1], 1.0f);
        put(1 + mi, r16, ah, 1.0f);
        WAIT_BAR(0);
        STAMP(21);
        const int h = lane >> 5, tk = 32 * mi + (lane & 31), pos = rb * 64 + tk;
        lds_ptr qp = lds + ((0 * 4 + hh) * 2 + h) * PL + (16 + tk) * 16;
        lds_ptr kp = lds + ((1 * 4 + hh) * 2 + h) * PL + (16 + tk) * 16;
        lds_ptr vp = lds + ((2 * 4 + hh) * 2 + h) * PL + (16 + tk) * 16;
        const u32x4 qv = *(LDSP(const u32x4))qp;
        float lg[8]; float m = -INFINITY;
#pragma unroll
        for (int w = 0; w < 8; ++w) {
            const u32x4 kv = *(LDSP(const u32x4))(kp - w * 32);
            float d = 0.f;
#pragma unroll
            for (int i = 0; i < 4; ++i) { const unsigned qa = qv[i], kb = kv[i]; d = __builtin_amdgcn_fdot2_f32_bf16(__builtin_bit_cast(bf2_t, qa), __builtin_bit_cast(bf2_t, kb), d, false); }
            auto rr = __builtin_amdgcn_permlane32_swap(__float_as_uint(d), __float_as_uint(d), false, false);
            d = __uint_as_float(rr[0]) + __uint_as_float(rr[1]);
            lg[w] = (pos - 2 * w >= 0) ? d : -INFINITY;
            m = fmaxf(m, lg[w]);
        }
        float l = 0.f, o[8] = {};
#pragma unroll
        for (int w = 0; w < 8; ++w) {
            const float p = __builtin_amdgcn_exp2f(lg[w] - m); l += p;
            const u32x4 vv = *(LDSP(const u32x4))(vp - w * 32);
#pragma unroll
            for (int i = 0; i < 4; ++i) { o[2 * i] += p * __uint_as_float(vv[i] << 16); o[2 * i + 1] += p * __uint_as_float(vv[i] & 0xffff0000u); }
        }
        const float rl = __builtin_amdgcn_rcpf(l);
        u32x4 ov = {cvtpk(o[0] * rl, o[1] * rl), cvtpk(o[2] * rl, o[3] * rl), cvtpk(o[4] * rl, o[5] * rl), cvtpk(o[6] * rl, o[7] * rl)};
        *(u32x4*)(ws + WS_OA + ((size_t)tile * 32 + head) * 1024 + lane * 16) = ov;
    }
}

#define A_EXP(b, i) "v_exp_f32 v[" #b "+" #i "], v[" #b "+" #i "]\n\t"
#define A_CVT(d, b, i) "v_cvt_pk_bf16_f32 v[" #d "], v[" #b "+" #i "], v[" #b "+" #i "+1]\n\t"
#define A_STEP(C, N, NL, NH_, j) \
    "s_waitcnt lgkmcnt(2)\n\t" \
    "v_mfma_f32_32x32x16_bf16 v[" #NL ":" #NH_ "], v[108:111], v[104:107], v[88:103]\n\t" \
    A_EXP(C, 0) "ds_read_b128 v[108:111], %[ka] offset:(" #j "+2)*1024\n\t" \
    A_EXP(C, 1) A_EXP(C, 2) A_EXP(C, 3) A_CVT(120, C, 0) A_EXP(C, 4) A_EXP(C, 5) A_CVT(121, C, 2) A_EXP(C, 6) A_EXP(C, 7) A_CVT(122, C, 4) \
    A_EXP(C, 8) A_CVT(123, C, 6) A_EXP(C, 9) "s_waitcnt lgkmcnt(2)\n\t" A_EXP(C, 10) \
    "v_mfma_f32_32x32x16_bf16 v[72:87], v[112:115], v[120:123], v[72:87]\n\t" \
    A_EXP(C, 11) "s_mov_b64 exec, %[mk]\n\t" "ds_read_b128 v[112:115], %[va] offset:(" #j "+1)*1024\n\t" "s_mov_b64 exec, -1\n\t" \
    A_CVT(124, C, 8) A_EXP(C, 12) A_EXP(C, 13) A_CVT(125, C, 10) A_EXP(C, 14) A_EXP(C, 15) A_CVT(126, C, 12) "s_waitcnt lgkmcnt(2)\n\t" A_CVT(127, C, 14) \
    "s_nop 1\n\t" \
    "v_mfma_f32_32x32x16_bf16 v[72:87], v[116:119], v[124:127], v[72:87]\n\t" \
    "s_mov_b64 exec, %[mk]\n\t" "ds_read_b128 v[116:119], %[va] offset:(" #j "+1)*1024+512\n\t" "s_mov_b64 exec, -1\n\t"

__device__ __forceinline__ f32x16 attn_keyloop(bf16x8 q, f32x16 negm, unsigned ka, unsigned va, int sync0, int sync1) {
    f32x16 o = {};
    const unsigned long long mk = 0x0000FFFF0000FFFFull;
    int it;
    asm volatile(
        "v_mov_b32 v112, 0x3f803f80\n\tv_mov_b32 v113, 0x3f803f80\n\tv_mov_b32 v114, 0x3f803f80\n\tv_mov_b32 v115, 0x3f803f80\n\t"
        "v_mov_b32 v116, 0x3f803f80\n\tv_mov_b32 v117, 0x3f803f80\n\tv_mov_b32 v118, 0x3f803f80\n\tv_mov_b32 v119, 0x3f803f80\n\t"
        "ds_read_b128 v[108:111], %[ka]\n\t"
        "s_mov_b32 %[it], 0\n\t"
        "s_waitcnt lgkmcnt(0)\n\t"
        "v_mfma_f32_32x32x16_bf16 v[40:55], v[108:111], v[104:107], v[88:103]\n\t"
        "ds_read_b128 v[108:111], %[ka] offset:1024\n\t"
        "s_mov_b64 exec, %[mk]\n\t"
        "ds_read_b128 v[112:115], %[va]\n\t"
        "ds_read_b128 v[116:119], %[va] offset:512\n\t"
        "s_mov_b64 exec, -1\n\t"
        "s_nop 7\n\t"
        "LOOP%=:\n\t"
        A_STEP(40, 56, 56, 71, 0) A_STEP(56, 40, 40, 55, 1) A_STEP(40, 56, 56, 71, 2) A_STEP(56, 40, 40, 55, 3)
        A_STEP(40, 56, 56, 71, 4) A_STEP(56, 40, 40, 55, 5)
        "s_cmp_lg_u32 %[it], %[s0]\n\t"
        "s_cbranch_scc1 SKA%=\n\t"
        "s_waitcnt vmcnt(8)\n\ts_barrier\n\t"
        "SKA%=:\n\t"
        "s_cmp_lg_u32 %[it], %[s1]\n\t"
        "s_cbranch_scc1 SKB%=\n\t"
        "s_waitcnt vmcnt(0)\n\ts_barrier\n\t"
        "SKB%=:\n\t"
        A_STEP(40, 56, 56, 71, 6) A_STEP(56, 40, 40, 55, 7)
        "v_add_u32 %[ka], 0x2000, %[ka]\n\t"
        "v_add_u32 %[va], 0x2000, %[va]\n\t"
        "s_add_u32 %[it], %[it], 1\n\t"
        "s_cmp_lt_u32 %[it], 8\n\t"
        "s_cbranch_scc1 LOOP%=\n\t"
        "s_waitcnt lgkmcnt(0)\n\t"
        "s_nop 7\n\ts_nop 7\n\t"
        : "+{v[72:87]}"(o), [ka] "+v"(ka), [va] "+v"(va), [it] "=&s"(it)
        : "{v[88:103]}"(negm), "{v[104:107]}"(q), [mk] "s"(mk), [s0] "s"(sync0), [s1] "s"(sync1)
        : "memory", "scc",
          "v40", "v41", "v42", "v43", "v44", "v45", "v46", "v47", "v48", "v49", "v50", "v51", "v52", "v53", "v54", "v55",
          "v56", "v57", "v58", "v59", "v60", "v61", "v62", "v63", "v64", "v65", "v66", "v67", "v68", "v69", "v70", "v71",
          "v108", "v109", "v110", "v111", "v112", "v113", "v114", "v115", "v116", "v117", "v118", "v119",
          "v120", "v121", "v122", "v123", "v124", "v125", "v126", "v127");
    return o;
}

__device__ __forceinline__ float rowmax16(const f32x16& s) {
    float a = fmaxf(fmaxf(s[0], s[1]), s[2]), b = fmaxf(fmaxf(s[3], s[4]), s[5]);
    a = fmaxf(fmaxf(a, s[6]), s[7]); b = fmaxf(fmaxf(b, s[8]), s[9]);
    a = fmaxf(fmaxf(a, s[10]), s[11]); b = fmaxf(fmaxf(b, s[12]), s[13]);
    a = fmaxf(fmaxf(a, s[14]), s[15]);
    return fmaxf(a, b);
}
__device__ __forceinline__ float halfmax(float m) {
    auto rr = __builtin_amdgcn_permlane32_swap(__float_as_uint(m), __float_as_uint(m), false, false);
    return fmaxf(__uint_as_float(rr[0]), __uint_as_float(rr[1]));
}

__device__ __forceinline__ void attn_phase(char* ws, int head, int qb, char* ldsg STAMP_ARG) {
    const int tid = threadIdx.x, lane = tid & 63; const int wid = rfl(tid >> 6);
    const unsigned lds0 = (unsigned)(uintptr_t)ldsg; lds_ptr lds = (lds_ptr)ldsg;
    const int qt = qb * 8 + wid;
    bf16x8 q;
    { const char* qp = ws + WS_Q + ((size_t)head * 64 + qt) * 1024 + lane * 16;
      asm volatile("global_load_dwordx4 %0, %1, off" : "=v"(q) : "v"(qp) : "memory"); }
    const char* ks = ws + WS_K + ((size_t)head * 64 + wid) * 1024 + lane * 16;
    const char* vs = ws + WS_V + ((size_t)head * 64 + wid) * 1024 + lane * 16;
#pragma unroll
    for (int c = 0; c < 8; ++c) {
        glds16(ks + c * 8192, rfl(lds0 + c * 8192 + wid * 1024));
        glds16(vs + c * 8192, rfl(lds0 + 65536 + c * 8192 + wid * 1024));
    }
    STAMP(1);
    asm volatile("s_waitcnt vmcnt(14)\n\ts_barrier" : "+v"(q) :: "memory");
    STAMP(2);
    float m;
    { f32x16 s = MFMA32(ldsfrag(lds + lane * 16), q, (f32x16){}); m = halfmax(rowmax16(s)); }
    const unsigned ka = lds0 + lane * 16, va = lds0 + 65536 + (lane >> 5) * 256 + (lane & 15) * 16;
    f32x16 negm;
#pragma unroll
    for (int i = 0; i < 16; ++i) negm[i] = -m;
    STAMP(4);
    f32x16 o = attn_keyloop(q, negm, ka, va, 0, 3);
    STAMP(5);
    bool ok = o[8] < 1.8446744e19f;
#pragma unroll
    for (int i = 0; i < 8; ++i) ok = ok && (fabsf(o[i]) <= 3.0e38f);
    if (__builtin_expect(!__all(ok), 0)) {
        m = -INFINITY;
        for (int t = 0; t < 64; ++t) { f32x16 s = MFMA32(ldsfrag(lds + t * 1024 + lane * 16), q, (f32x16){}); m = fmaxf(m, rowmax16(s)); }
        m = halfmax(m);
#pragma unroll
        for (int i = 0; i < 16; ++i) negm[i] = -m;
        o = attn_keyloop(q, negm, ka, va, -1, -1);
    }
    const float rl = __builtin_amdgcn_rcpf(o[8]);
    u32x4 ov = {cvtpk(o[0] * rl, o[1] * rl), cvtpk(o[2] * rl, o[3] * rl), cvtpk(o[4] * rl, o[5] * rl), cvtpk(o[6] * rl, o[7] * rl)};
    *(u32x4*)(ws + WS_OB + ((size_t)qt * 32 + head) * 1024 + lane * 16) = ov;
}

__device__ __forceinline__ void wo_issue(const u16* __restrict__ Wo, int cb, unsigned ldsbase) {
    const int lane = threadIdx.x & 63; const int wid = rfl(threadIdx.x >> 6);
    const u16* src = Wo + (size_t)(wid * 8 + (lane >> 3)) * DM + cb * 64 + (((lane & 7) ^ (((lane >> 4) & 3) << 1)) * 8);
#pragma unroll
    for (int i = 0; i < 8; ++i) glds16(src + (size_t)i * 64 * DM, rfl(ldsbase + i * 8192 + wid * 1024));
}
__device__ __forceinline__ void oproj_compute(const bf16x8 (&of)[8], lds_ptr wimg, lds_ptr red, u16* out, int rb, int cb, int br) {
    const int tid = threadIdx.x, lane = tid & 63; const int wid = rfl(tid >> 6);
    const int rg = wid & 3, kh = wid >> 2, g = lane >> 4, r16 = lane & 15;
    const int q4 = r16 >> 2, phi = ((g & 1) << 1) | (q4 >> 1);
    const int rowb = (kh * 256 + 16 * (g >> 1) + 4 * (g & 1) + q4) * 128 + (lane & 3) * 8;
    f32x4 acc[4] = {};
#pragma unroll
    for (int s = 0; s < 8; ++s)
#pragma unroll
        for (int nb = 0; nb < 4; ++nb)
            acc[nb] = MFMA16(trfrag(wimg + rowb + s * 4096 + ((nb ^ phi) * 32), 1024), of[s], acc[nb]);
#pragma unroll
    for (int nb = 0; nb < 4; ++nb) *(LDSP(f32x4))(red + kh * 16384 + (rg * 16 + r16) * 256 + (((nb * 4 + g) ^ (r16 & 7)) * 16)) = acc[nb];
    asm volatile("s_waitcnt lgkmcnt(0)\n\ts_barrier" ::: "memory");
    const int row = tid >> 3, ch = tid & 7;
    const int c0 = row * 256 + (((2 * ch) ^ (row & 7)) * 16);
    f32x4 a0 = *(LDSP(const f32x4))(red + c0), a1 = *(LDSP(const f32x4))(red + (c0 ^ 16)), b0 = *(LDSP(const f32x4))(red + 16384 + c0), b1 = *(LDSP(const f32x4))(red + 16384 + (c0 ^ 16));
    a0 += b0; a1 += b1;
    u32x4 ov = {cvtpk(a0[0], a0[1]), cvtpk(a0[2], a0[3]), cvtpk(a1[0], a1[1]), cvtpk(a1[2], a1[3])};
    *(u32x4*)(out + (size_t)(rb * 64 + row) * 1024 + br * 512 + cb * 64 + ch * 8) = ov;
}
__device__ __forceinline__ const char* ofrag_ptr(const char* O, int rb) {
    const int lane = threadIdx.x & 63; const int wid = rfl(threadIdx.x >> 6);
    const int rg = wid & 3, kh = wid >> 2, g = lane >> 4, r16 = lane & 15;
    return O + ((size_t)(rb * 2 + (rg >> 1)) * 32 + kh * 16 + (g >> 1)) * 1024 + (g & 1) * 512 + ((rg & 1) * 16 + r16) * 16;
}

__global__ __launch_bounds__(512, 2) void k_qkv(const u16* xa, const u16* wa, const u16* xb, const u16* wb, char* ws) {
    extern __shared__ __attribute__((aligned(16))) char lds[];
    const int b = blockIdx.x, xcd = b & 7, idx = b >> 3;
    const int rb = (xcd >> 1) * 8 + (idx & 7), cg = (xcd & 1) * 4 + (idx >> 3);
    STAMP_DECL; STAMP(0);
    qkv_phase<false>(xb, wb, ws, rb, cg, lds STAMP_PASS);
    asm volatile("s_waitcnt lgkmcnt(0)\n\ts_barrier" ::: "memory");
    STAMP(10);
    qkv_phase<true>(xa, wa, ws, rb, cg, lds STAMP_PASS);
    STAMP(22); STAMP_OUT(0);
}
__global__ __launch_bounds__(512, 2) void k_attn(char* ws) {
    extern __shared__ __attribute__((aligned(16))) char lds[];
    const int b = blockIdx.x, xcd = b & 7, idx = b >> 3;
    STAMP_DECL; STAMP(0);
    attn_phase(ws, xcd * 4 + (idx & 3), idx >> 2, lds STAMP_PASS);
    STAMP(6); STAMP_OUT(1);
}
__global__ __launch_bounds__(512, 2) void k_oproj(const u16* woa, const u16* wob, char* ws, u16* out) {
    extern __shared__ __attribute__((aligned(16))) char lds[];
    const int b = blockIdx.x, xcd = b & 7, idx = b >> 3;
    const int rb = xcd * 4 + (idx & 3), cb = idx >> 2;
    const unsigned lds0 = (unsigned)(uintptr_t)lds;
    bf16x8 fa[8], fb[8];
    STAMP_DECL; STAMP(0);
    const char* pa = ofrag_ptr(ws + WS_OA, rb); const char* pb = ofrag_ptr(ws + WS_OB, rb);
#pragma unroll
    for (int s = 0; s < 8; ++s) asm volatile("global_load_dwordx4 %0, %1, off offset:%2" : "=v"(fa[s]) : "v"(pa + (s >> 1) * 4096), "n"((s & 1) * 2048) : "memory");
    wo_issue(woa, cb, lds0);
#pragma unroll
    for (int s = 0; s < 8; ++s) asm volatile("global_load_dwordx4 %0, %1, off offset:%2" : "=v"(fb[s]) : "v"(pb + (s >> 1) * 4096), "n"((s & 1) * 2048) : "memory");
    wo_issue(wob, cb, lds0 + 65536);
    STAMP(1);
    asm volatile("s_waitcnt vmcnt(16)\n\ts_barrier" : "+v"(fa[0]), "+v"(fa[1]), "+v"(fa[2]), "+v"(fa[3]), "+v"(fa[4]), "+v"(fa[5]), "+v"(fa[6]), "+v"(fa[7]) :: "memory");
    STAMP(2);
    oproj_compute(fa, (lds_ptr)lds, (lds_ptr)lds + 131072, out, rb, cb, 0);
    STAMP(3);
    asm volatile("s_waitcnt vmcnt(1) lgkmcnt(0)\n\ts_barrier" : "+v"(fb[0]), "+v"(fb[1]), "+v"(fb[2]), "+v"(fb[3]), "+v"(fb[4]), "+v"(fb[5]), "+v"(fb[6]), "+v"(fb[7]) :: "memory");
    STAMP(4);
    oproj_compute(fb, (lds_ptr)lds + 65536, (lds_ptr)lds, out, rb, cb, 1);
    STAMP(5); STAMP_OUT(2);
}

extern "C" void kernel_launch(void* const* d_in, const int* in_sizes, int n_in, void* d_out, int out_size, void* d_ws, size_t ws_size, hipStream_t stream) {
    const u16* xa = (const u16*)d_in[0]; const u16* wa = (const u16*)d_in[1]; const u16* woa = (const u16*)d_in[2];
    const u16* xb = (const u16*)d_in[3]; const u16* wb = (const u16*)d_in[4]; const u16* wob = (const u16*)d_in[5];
    (void)hipFuncSetAttribute((const void*)k_qkv, hipFuncAttributeMaxDynamicSharedMemorySize, 163840);
    (void)hipFuncSetAttribute((const void*)k_attn, hipFuncAttributeMaxDynamicSharedMemorySize, 163840);
    (void)hipFuncSetAttribute((const void*)k_oproj, hipFuncAttributeMaxDynamicSharedMemorySize, 163840);
    k_qkv<<<256, 512, 4 * 34816, stream>>>(xa, wa, xb, wb, (char*)d_ws);
    k_attn<<<256, 512, 131072 + 4096, stream>>>((char*)d_ws);
    k_oproj<<<256, 512, 163840, stream>>>(woa, wob, (char*)d_ws, (u16*)d_out);
}
```

```cpp
#include <hip/hip_runtime.h>
#include <hip/hip_bf16.h>
#include <stdint.h>

typedef short bf16x8 __attribute__((ext_vector_type(8)));
typedef short s16x4 __attribute__((ext_vector_type(4)));
typedef float f32x2 __attribute__((ext_vector_type(2)));
typedef float f32x4 __attribute__((ext_vector_type(4)));
typedef float f32x16 __attribute__((ext_vector_type(16)));
typedef unsigned u32x4 __attribute__((ext_vector_type(4)));
typedef unsigned u32x2 __attribute__((ext_vector_type(2)));
typedef __bf16 bf2_t __attribute__((ext_vector_type(2)));
typedef unsigned short u16;
typedef unsigned long long u64;
#define LDSP(T) __attribute__((address_space(3))) T*
typedef LDSP(char) lds_ptr;
typedef __attribute__((address_space(1))) u64 gu64;

constexpr int DM = 512;
constexpr float QSCALE = 0.25f * 1.4426950408889634f;
constexpr unsigned WS_Q = 0, WS_K = 2u << 20, WS_V = 4u << 20, WS_OA = 6u << 20, WS_OB = 8u << 20, WS_FL = 10u << 20, WS_END = WS_FL + 65536;
constexpr unsigned FL_B1 = WS_FL + 4096, FL_A = FL_B1 + 16384, FL_B2 = FL_A + 16384;
constexpr int STAGE = 34816, OFF_W = 0, OFF_X = 24576, OFF_H = 32768, L_PLANES = 131072, L_V = 65536, L_COMB = 131072, L_RED = 131072, L_CTL = 163840 - 128, LDS_BYTES = 163840;


#define MFMA16(a, b, c) __builtin_amdgcn_mfma_f32_16x16x32_bf16(a, b, c, 0, 0, 0)
#define MFMA32(a, b, c) __builtin_amdgcn_mfma_f32_32x32x16_bf16(a, b, c, 0, 0, 0)
#define WAIT_BAR(N) asm volatile("s_waitcnt vmcnt(" #N ") lgkmcnt(0)\n\ts_barrier" ::: "memory")
#define LDS_BAR() asm volatile("s_waitcnt lgkmcnt(0)\n\ts_barrier" ::: "memory")
#define SBAR() __builtin_amdgcn_sched_barrier(0)
#define RLX_AGENT __ATOMIC_RELAXED, __HIP_MEMORY_SCOPE_AGENT

__device__ __forceinline__ unsigned cvtpk(float lo, float hi) { return __builtin_bit_cast(unsigned, __builtin_convertvector((f32x2){lo, hi}, bf2_t)); }
template <int IMM> __device__ __forceinline__ void dma16(const void* base, unsigned voff, unsigned dst) {
    asm volatile("s_mov_b32 m0, %2\n\ts_nop 0\n\tglobal_load_lds_dwordx4 %0, %1 offset:%3" :: "v"(voff), "s"(base), "s"(dst - IMM), "n"(IMM) : "memory"); }
template <int IMM> __device__ __forceinline__ void dma16_dev(const void* base, unsigned voff, unsigned dst) {
    asm volatile("s_mov_b32 m0, %2\n\ts_nop 0\n\tglobal_load_lds_dwordx4 %0, %1 offset:%3 sc1" :: "v"(voff), "s"(base), "s"(dst - IMM), "n"(IMM) : "memory"); }
template <int IMM> __device__ __forceinline__ void dma4(const void* base, unsigned voff, unsigned dst) {
    asm volatile("s_mov_b32 m0, %2\n\ts_nop 0\n\tglobal_load_lds_dword %0, %1 offset:%3" :: "v"(voff), "s"(base), "s"(dst - IMM), "n"(IMM) : "memory"); }
template <int IMM> __device__ __forceinline__ void ld16_dev(bf16x8& r, const void* base, unsigned voff) {
    asm volatile("global_load_dwordx4 %0, %1, %2 offset:%3 sc1" : "=v"(r) : "v"(voff), "s"(base), "n"(IMM) : "memory"); }
typedef short v4i16_t __attribute__((ext_vector_type(4)));
__device__ __forceinline__ s16x4 trread(lds_ptr p) { return __builtin_bit_cast(s16x4, __builtin_amdgcn_ds_read_tr16_b64_v4i16((LDSP(v4i16_t))p)); }
__device__ __forceinline__ bf16x8 trfrag(lds_ptr p, int second) {
    s16x4 a = trread(p), b = trread(p + second);
    return (bf16x8){a[0], a[1], a[2], a[3], b[0], b[1], b[2], b[3]}; }
__device__ __forceinline__ bf16x8 ldsfrag(lds_ptr p) { return *(LDSP(const bf16x8))p; }
__device__ __forceinline__ unsigned rfl(unsigned x) { return __builtin_amdgcn_readfirstlane(x); }
__device__ __forceinline__ void spin_until(const char* word, u64 ep) {
    const u64 t0 = __builtin_amdgcn_s_memrealtime();
    while (!__all(__hip_atomic_load((gu64*)word, RLX_AGENT) == ep)) {
        __builtin_amdgcn_s_sleep(2);
        if (__builtin_amdgcn_s_memrealtime() - t0 > 5000000ull) break;
    }
    asm volatile("" ::: "memory");
}
__device__ __forceinline__ u32x4 pack_pair(f32x4 a, f32x4 b, float s) {
    unsigned a0 = cvtpk(a[0] * s, a[1] * s), a1 = cvtpk(a[2] * s, a[3] * s), b0 = cvtpk(b[0] * s, b[1] * s), b1 = cvtpk(b[2] * s, b[3] * s);
    auto r0 = __builtin_amdgcn_permlane32_swap(a0, b0, false, false);
    auto r1 = __builtin_amdgcn_permlane32_swap(a1, b1, false, false);
    return (u32x4){r0[0], r1[0], r0[1], r1[1]};
}
__device__ __forceinline__ float rowmax16(const f32x16& s) {
    float a = fmaxf(fmaxf(s[0], s[1]), s[2]), b = fmaxf(fmaxf(s[3], s[4]), s[5]);
    a = fmaxf(fmaxf(a, s[6]), s[7]); b = fmaxf(fmaxf(b, s[8]), s[9]);
    a = fmaxf(fmaxf(a, s[10]), s[11]); b = fmaxf(fmaxf(b, s[12]), s[13]);
    a = fmaxf(fmaxf(a, s[14]), s[15]);
    return fmaxf(a, b);
}
__device__ __forceinline__ float halfmax(float m) {
    auto rr = __builtin_amdgcn_permlane32_swap(__float_as_uint(m), __float_as_uint(m), false, false);
    return fmaxf(__uint_as_float(rr[0]), __uint_as_float(rr[1]));
}
__device__ __forceinline__ f32x16 splat16(float v) { f32x16 r;
#pragma unroll
    for (int i = 0; i < 16; ++i) r[i] = v;
    return r; }

__device__ __forceinline__ void attn_keyloop(f32x16& oa, f32x16& ob, bf16x8 qa, bf16x8 qb, f32x16 na, f32x16 nb, unsigned ka, unsigned va, unsigned ones,
                                             int s0, int s1, int wid, const void* fa, unsigned long long ep, const void* pa, unsigned long long& pv) {
    unsigned long long mk = 0x0000FFFF0000FFFFull; int it;
    asm volatile(
        "v_mov_b32 v148, %[ones]\n\t"
        "v_mov_b32 v149, %[ones]\n\t"
        "v_mov_b32 v150, %[ones]\n\t"
        "v_mov_b32 v151, %[ones]\n\t"
        "v_mov_b32 v152, %[ones]\n\t"
        "v_mov_b32 v153, %[ones]\n\t"
        "v_mov_b32 v154, %[ones]\n\t"
        "v_mov_b32 v155, %[ones]\n\t"
        "v_mov_b32 v160, 0\n\t"
        "v_mov_b32 v161, 0\n\t"
        "v_mov_b32 v162, 0\n\t"
        "v_mov_b32 v70, 0\n\t"
        "v_mov_b32 v71, 0\n\t"
        "s_mov_b32 %[it], 0\n\t"
        "ds_read_b128 v[144:147], %[ka] offset:0\n\t"
        "s_mov_b64 exec, %[mk]\n\t"
        "ds_read_b128 v[152:155], %[va] offset:512\n\t"
        "s_mov_b64 exec, -1\n\t"
        "s_waitcnt lgkmcnt(0)\n\t"
        "v_mfma_f32_32x32x16_bf16 v[40:55], v[144:147], v[136:139], v[104:119]\n\t"
        "s_mov_b64 exec, %[mk]\n\t"
        "ds_read_b128 v[148:151], %[va] offset:0\n\t"
        "s_mov_b64 exec, -1\n\t"
        "s_nop 7\n\t"
        "s_nop 3\n\t"
        "LOOP%=:\n\t"
        "v_exp_f32 v40, v40\n\t"
        "v_cvt_pk_bf16_f32 v163, v70, v71\n\t"
        "v_mfma_f32_32x32x16_bf16 v[56:71], v[144:147], v[140:143], v[120:135]\n\t"
        "v_exp_f32 v41, v41\n\t"
        "ds_read_b128 v[144:147], %[ka] offset:1024\n\t"
        "v_exp_f32 v42, v42\n\t"
        "v_exp_f32 v43, v43\n\t"
        "v_cvt_pk_bf16_f32 v156, v40, v41\n\t"
        "v_exp_f32 v44, v44\n\t"
        "v_exp_f32 v45, v45\n\t"
        "v_cvt_pk_bf16_f32 v157, v42, v43\n\t"
        "v_mfma_f32_32x32x16_bf16 v[88:103], v[152:155], v[160:163], v[88:103]\n\t"
        "v_exp_f32 v46, v46\n\t"
        "s_mov_b64 exec, %[mk]\n\t"
        "ds_read_b128 v[152:155], %[va] offset:512\n\t"
        "s_mov_b64 exec, -1\n\t"
        "v_exp_f32 v47, v47\n\t"
        "v_cvt_pk_bf16_f32 v158, v44, v45\n\t"
        "v_exp_f32 v48, v48\n\t"
        "v_cvt_pk_bf16_f32 v159, v46, v47\n\t"
        "v_exp_f32 v49, v49\n\t"
        "v_exp_f32 v50, v50\n\t"
        "s_waitcnt lgkmcnt(2)\n\t"
        "v_exp_f32 v51, v51\n\t"
        "v_mfma_f32_32x32x16_bf16 v[72:87], v[148:151], v[156:159], v[72:87]\n\t"
        "v_cvt_pk_bf16_f32 v160, v48, v49\n\t"
        "v_exp_f32 v52, v52\n\t"
        "v_exp_f32 v53, v53\n\t"
        "v_cvt_pk_bf16_f32 v161, v50, v51\n\t"
        "v_exp_f32 v54, v54\n\t"
        "v_exp_f32 v55, v55\n\t"
        "v_cvt_pk_bf16_f32 v162, v52, v53\n\t"
        "v_exp_f32 v56, v56\n\t"
        "s_waitcnt lgkmcnt(1)\n\t"
        "v_cvt_pk_bf16_f32 v163, v54, v55\n\t"
        "v_mfma_f32_32x32x16_bf16 v[40:55], v[144:147], v[136:139], v[104:119]\n\t"
        "v_exp_f32 v57, v57\n\t"
        "v_exp_f32 v58, v58\n\t"
        "v_exp_f32 v59, v59\n\t"
        "v_cvt_pk_bf16_f32 v156, v56, v57\n\t"
        "v_exp_f32 v60, v60\n\t"
        "s_waitcnt lgkmcnt(0)\n\t"
        "v_exp_f32 v61, v61\n\t"
        "v_cvt_pk_bf16_f32 v157, v58, v59\n\t"
        "v_mfma_f32_32x32x16_bf16 v[72:87], v[152:155], v[160:163], v[72:87]\n\t"
        "v_exp_f32 v62, v62\n\t"
        "v_exp_f32 v63, v63\n\t"
        "v_cvt_pk_bf16_f32 v158, v60, v61\n\t"
        "v_exp_f32 v64, v64\n\t"
        "v_cvt_pk_bf16_f32 v159, v62, v63\n\t"
        "v_exp_f32 v65, v65\n\t"
        "v_exp_f32 v66, v66\n\t"
        "v_exp_f32 v67, v67\n\t"
        "v_mfma_f32_32x32x16_bf16 v[88:103], v[148:151], v[156:159], v[88:103]\n\t"
        "v_cvt_pk_bf16_f32 v160, v64, v65\n\t"
        "v_exp_f32 v68, v68\n\t"
        "s_mov_b64 exec, %[mk]\n\t"
        "ds_read_b128 v[148:151], %[va] offset:1024\n\t"
        "s_mov_b64 exec, -1\n\t"
        "v_exp_f32 v69, v69\n\t"
        "v_cvt_pk_bf16_f32 v161, v66, v67\n\t"
        "v_exp_f32 v70, v70\n\t"
        "v_exp_f32 v71, v71\n\t"
        "v_cvt_pk_bf16_f32 v162, v68, v69\n\t"
        "v_exp_f32 v40, v40\n\t"
        "v_cvt_pk_bf16_f32 v163, v70, v71\n\t"
        "v_mfma_f32_32x32x16_bf16 v[56:71], v[144:147], v[140:143], v[120:135]\n\t"
        "v_exp_f32 v41, v41\n\t"
        "ds_read_b128 v[144:147], %[ka] offset:2048\n\t"
        "v_exp_f32 v42, v42\n\t"
        "v_exp_f32 v43, v43\n\t"
        "v_cvt_pk_bf16_f32 v156, v40, v41\n\t"
        "v_exp_f32 v44, v44\n\t"
        "v_exp_f32 v45, v45\n\t"
        "v_cvt_pk_bf16_f32 v157, v42, v43\n\t"
        "v_mfma_f32_32x32x16_bf16 v[88:103], v[152:155], v[160:163], v[88:103]\n\t"
        "v_exp_f32 v46, v46\n\t"
        "s_mov_b64 exec, %[mk]\n\t"
        "ds_read_b128 v[152:155], %[va] offset:1536\n\t"
        "s_mov_b64 exec, -1\n\t"
        "v_exp_f32 v47, v47\n\t"
        "v_cvt_pk_bf16_f32 v158, v44, v45\n\t"
        "v_exp_f32 v48, v48\n\t"
        "v_cvt_pk_bf16_f32 v159, v46, v47\n\t"
        "v_exp_f32 v49, v49\n\t"
        "v_exp_f32 v50, v50\n\t"
        "s_waitcnt lgkmcnt(2)\n\t"
        "v_exp_f32 v51, v51\n\t"
        "v_mfma_f32_32x32x16_bf16 v[72:87], v[148:151], v[156:159], v[72:87]\n\t"
        "v_cvt_pk_bf16_f32 v160, v48, v49\n\t"
        "v_exp_f32 v52, v52\n\t"
        "v_exp_f32 v53, v53\n\t"
        "v_cvt_pk_bf16_f32 v161, v50, v51\n\t"
        "v_exp_f32 v54, v54\n\t"
        "v_exp_f32 v55, v55\n\t"
        "v_cvt_pk_bf16_f32 v162, v52, v53\n\t"
        "v_exp_f32 v56, v56\n\t"
        "s_waitcnt lgkmcnt(1)\n\t"
        "v_cvt_pk_bf16_f32 v163, v54, v55\n\t"
        "v_mfma_f32_32x32x16_bf16 v[40:55], v[144:147], v[136:139], v[104:119]\n\t"
        "v_exp_f32 v57, v57\n\t"
        "v_exp_f32 v58, v58\n\t"
        "v_exp_f32 v59, v59\n\t"
        "v_cvt_pk_bf16_f32 v156, v56, v57\n\t"
        "v_exp_f32 v60, v60\n\t"
        "s_waitcnt lgkmcnt(0)\n\t"
        "v_exp_f32 v61, v61\n\t"
        "v_cvt_pk_bf16_f32 v157, v58, v59\n\t"
        "v_mfma_f32_32x32x16_bf16 v[72:87], v[152:155], v[160:163], v[72:87]\n\t"
        "v_exp_f32 v62, v62\n\t"
        "v_exp_f32 v63, v63\n\t"
        "v_cvt_pk_bf16_f32 v158, v60, v61\n\t"
        "v_exp_f32 v64, v64\n\t"
        "v_cvt_pk_bf16_f32 v159, v62, v63\n\t"
        "v_exp_f32 v65, v65\n\t"
        "v_exp_f32 v66, v66\n\t"
        "v_exp_f32 v67, v67\n\t"
        "v_mfma_f32_32x32x16_bf16 v[88:103], v[148:151], v[156:159], v[88:103]\n\t"
        "v_cvt_pk_bf16_f32 v160, v64, v65\n\t"
        "v_exp_f32 v68, v68\n\t"
        "s_mov_b64 exec, %[mk]\n\t"
        "ds_read_b128 v[148:151], %[va] offset:2048\n\t"
        "s_mov_b64 exec, -1\n\t"
        "v_exp_f32 v69, v69\n\t"
        "v_cvt_pk_bf16_f32 v161, v66, v67\n\t"
        "v_exp_f32 v70, v70\n\t"
        "v_exp_f32 v71, v71\n\t"
        "v_cvt_pk_bf16_f32 v162, v68, v69\n\t"
        "v_exp_f32 v40, v40\n\t"
        "v_cvt_pk_bf16_f32 v163, v70, v71\n\t"
        "v_mfma_f32_32x32x16_bf16 v[56:71], v[144:147], v[140:143], v[120:135]\n\t"
        "v_exp_f32 v41, v41\n\t"
        "ds_read_b128 v[144:147], %[ka] offset:3072\n\t"
        "v_exp_f32 v42, v42\n\t"
        "v_exp_f32 v43, v43\n\t"
        "v_cvt_pk_bf16_f32 v156, v40, v41\n\t"
        "v_exp_f32 v44, v44\n\t"
        "v_exp_f32 v45, v45\n\t"
        "v_cvt_pk_bf16_f32 v157, v42, v43\n\t"
        "v_mfma_f32_32x32x16_bf16 v[88:103], v[152:155], v[160:163], v[88:103]\n\t"
        "v_exp_f32 v46, v46\n\t"
        "s_mov_b64 exec, %[mk]\n\t"
        "ds_read_b128 v[152:155], %[va] offset:2560\n\t"
        "s_mov_b64 exec, -1\n\t"
        "v_exp_f32 v47, v47\n\t"
        "v_cvt_pk_bf16_f32 v158, v44, v45\n\t"
        "v_exp_f32 v48, v48\n\t"
        "v_cvt_pk_bf16_f32 v159, v46, v47\n\t"
        "v_exp_f32 v49, v49\n\t"
        "v_exp_f32 v50, v50\n\t"
        "s_waitcnt lgkmcnt(2)\n\t"
        "v_exp_f32 v51, v51\n\t"
        "v_mfma_f32_32x32x16_bf16 v[72:87], v[148:151], v[156:159], v[72:87]\n\t"
        "v_cvt_pk_bf16_f32 v160, v48, v49\n\t"
        "v_exp_f32 v52, v52\n\t"
        "v_exp_f32 v53, v53\n\t"
        "v_cvt_pk_bf16_f32 v161, v50, v51\n\t"
        "v_exp_f32 v54, v54\n\t"
        "v_exp_f32 v55, v55\n\t"
        "v_cvt_pk_bf16_f32 v162, v52, v53\n\t"
        "v_exp_f32 v56, v56\n\t"
        "s_waitcnt lgkmcnt(1)\n\t"
        "v_cvt_pk_bf16_f32 v163, v54, v55\n\t"
        "v_mfma_f32_32x32x16_bf16 v[40:55], v[144:147], v[136:139], v[104:119]\n\t"
        "v_exp_f32 v57, v57\n\t"
        "v_exp_f32 v58, v58\n\t"
        "v_exp_f32 v59, v59\n\t"
        "v_cvt_pk_bf16_f32 v156, v56, v57\n\t"
        "v_exp_f32 v60, v60\n\t"
        "s_waitcnt lgkmcnt(0)\n\t"
        "v_exp_f32 v61, v61\n\t"
        "v_cvt_pk_bf16_f32 v157, v58, v59\n\t"
        "v_mfma_f32_32x32x16_bf16 v[72:87], v[152:155], v[160:163], v[72:87]\n\t"
        "v_exp_f32 v62, v62\n\t"
        "v_exp_f32 v63, v63\n\t"
        "v_cvt_pk_bf16_f32 v158, v60, v61\n\t"
        "v_exp_f32 v64, v64\n\t"
        "v_cvt_pk_bf16_f32 v159, v62, v63\n\t"
        "v_exp_f32 v65, v65\n\t"
        "v_exp_f32 v66, v66\n\t"
        "v_exp_f32 v67, v67\n\t"
        "v_mfma_f32_32x32x16_bf16 v[88:103], v[148:151], v[156:159], v[88:103]\n\t"
        "v_cvt_pk_bf16_f32 v160, v64, v65\n\t"
        "v_exp_f32 v68, v68\n\t"
        "s_mov_b64 exec, %[mk]\n\t"
        "ds_read_b128 v[148:151], %[va] offset:3072\n\t"
        "s_mov_b64 exec, -1\n\t"
        "v_exp_f32 v69, v69\n\t"
        "v_cvt_pk_bf16_f32 v161, v66, v67\n\t"
        "v_exp_f32 v70, v70\n\t"
        "v_exp_f32 v71, v71\n\t"
        "v_cvt_pk_bf16_f32 v162, v68, v69\n\t"
        "s_cmp_lg_u32 %[it], %[s0]\n\t"
        "s_cbranch_scc1 SKA%=\n\t"
        "s_waitcnt vmcnt(0)\n\t"
        "s_barrier\n\t"
        "s_cmp_lg_u32 %[wid], 0\n\t"
        "s_cbranch_scc1 SKA%=\n\t"
        "s_mov_b64 exec, 1\n\t"
        "global_store_dwordx2 %[fa], %[ep], off sc1\n\t"
        "s_mov_b64 exec, -1\n\t"
        "SKA%=:\n\t"
        "s_cmp_lg_u32 %[it], %[s1]\n\t"
        "s_cbranch_scc1 SKB%=\n\t"
        "global_load_dwordx2 %[pv], %[pa], off sc1\n\t"
        "SKB%=:\n\t"
        "v_exp_f32 v40, v40\n\t"
        "v_cvt_pk_bf16_f32 v163, v70, v71\n\t"
        "v_mfma_f32_32x32x16_bf16 v[56:71], v[144:147], v[140:143], v[120:135]\n\t"
        "v_exp_f32 v41, v41\n\t"
        "ds_read_b128 v[144:147], %[ka] offset:4096\n\t"
        "v_exp_f32 v42, v42\n\t"
        "v_exp_f32 v43, v43\n\t"
        "v_cvt_pk_bf16_f32 v156, v40, v41\n\t"
        "v_exp_f32 v44, v44\n\t"
        "v_exp_f32 v45, v45\n\t"
        "v_cvt_pk_bf16_f32 v157, v42, v43\n\t"
        "v_mfma_f32_32x32x16_bf16 v[88:103], v[152:155], v[160:163], v[88:103]\n\t"
        "v_exp_f32 v46, v46\n\t"
        "s_mov_b64 exec, %[mk]\n\t"
        "ds_read_b128 v[152:155], %[va] offset:3584\n\t"
        "s_mov_b64 exec, -1\n\t"
        "v_exp_f32 v47, v47\n\t"
        "v_cvt_pk_bf16_f32 v158, v44, v45\n\t"
        "v_exp_f32 v48, v48\n\t"
        "v_cvt_pk_bf16_f32 v159, v46, v47\n\t"
        "v_exp_f32 v49, v49\n\t"
        "v_exp_f32 v50, v50\n\t"
        "s_waitcnt lgkmcnt(2)\n\t"
        "v_exp_f32 v51, v51\n\t"
        "v_mfma_f32_32x32x16_bf16 v[72:87], v[148:151], v[156:159], v[72:87]\n\t"
        "v_cvt_pk_bf16_f32 v160, v48, v49\n\t"
        "v_exp_f32 v52, v52\n\t"
        "v_exp_f32 v53, v53\n\t"
        "v_cvt_pk_bf16_f32 v161, v50, v51\n\t"
        "v_exp_f32 v54, v54\n\t"
        "v_exp_f32 v55, v55\n\t"
        "v_cvt_pk_bf16_f32 v162, v52, v53\n\t"
        "v_exp_f32 v56, v56\n\t"
        "s_waitcnt lgkmcnt(1)\n\t"
        "v_cvt_pk_bf16_f32 v163, v54, v55\n\t"
        "v_mfma_f32_32x32x16_bf16 v[40:55], v[144:147], v[136:139], v[104:119]\n\t"
        "v_exp_f32 v57, v57\n\t"
        "v_exp_f32 v58, v58\n\t"
        "v_exp_f32 v59, v59\n\t"
        "v_cvt_pk_bf16_f32 v156, v56, v57\n\t"
        "v_exp_f32 v60, v60\n\t"
        "s_waitcnt lgkmcnt(0)\n\t"
        "v_exp_f32 v61, v61\n\t"
        "v_cvt_pk_bf16_f32 v157, v58, v59\n\t"
        "v_mfma_f32_32x32x16_bf16 v[72:87], v[152:155], v[160:163], v[72:87]\n\t"
        "v_exp_f32 v62, v62\n\t"
        "v_exp_f32 v63, v63\n\t"
        "v_cvt_pk_bf16_f32 v158, v60, v61\n\t"
        "v_exp_f32 v64, v64\n\t"
        "v_cvt_pk_bf16_f32 v159, v62, v63\n\t"
        "v_exp_f32 v65, v65\n\t"
        "v_exp_f32 v66, v66\n\t"
        "v_exp_f32 v67, v67\n\t"
        "v_mfma_f32_32x32x16_bf16 v[88:103], v[148:151], v[156:159], v[88:103]\n\t"
        "v_cvt_pk_bf16_f32 v160, v64, v65\n\t"
        "v_exp_f32 v68, v68\n\t"
        "s_mov_b64 exec, %[mk]\n\t"
        "ds_read_b128 v[148:151], %[va] offset:4096\n\t"
        "s_mov_b64 exec, -1\n\t"
        "v_exp_f32 v69, v69\n\t"
        "v_cvt_pk_bf16_f32 v161, v66, v67\n\t"
        "v_exp_f32 v70, v70\n\t"
        "v_exp_f32 v71, v71\n\t"
        "v_cvt_pk_bf16_f32 v162, v68, v69\n\t"
        "v_add_u32 %[ka], 4096, %[ka]\n\t"
        "v_add_u32 %[va], 4096, %[va]\n\t"
        "s_add_u32 %[it], %[it], 1\n\t"
        "s_cmp_lt_u32 %[it], 8\n\t"
        "s_cbranch_scc1 LOOP%=\n\t"
        "v_cvt_pk_bf16_f32 v163, v70, v71\n\t"
        "s_waitcnt vmcnt(0) lgkmcnt(0)\n\t"
        "s_nop 1\n\t"
        "v_mfma_f32_32x32x16_bf16 v[88:103], v[152:155], v[160:163], v[88:103]\n\t"
        "s_nop 7\n\t"
        "s_nop 7\n\t"
        : "+{v[72:87]}"(oa), "+{v[88:103]}"(ob), [ka] "+v"(ka), [va] "+v"(va), [it] "=&s"(it), [pv] "+v"(pv)
        : "{v[104:119]}"(na), "{v[120:135]}"(nb), "{v[136:139]}"(qa), "{v[140:143]}"(qb), [mk] "s"(mk), [ones] "v"(ones),
          [s0] "s"(s0), [s1] "s"(s1), [wid] "s"(wid), [fa] "v"(fa), [ep] "v"(ep), [pa] "v"(pa)
        : "memory", "scc", "v40", "v41", "v42", "v43", "v44", "v45", "v46", "v47", "v48", "v49", "v50", "v51", "v52", "v53", "v54", "v55", "v56", "v57", "v58", "v59", "v60", "v61", "v62", "v63", "v64", "v65", "v66", "v67", "v68", "v69", "v70", "v71", "v144", "v145", "v146", "v147", "v148", "v149", "v150", "v151", "v152", "v153", "v154", "v155", "v156", "v157", "v158", "v159", "v160", "v161", "v162", "v163");
}


__global__ __launch_bounds__(512, 2) void fused_mha(const u16* __restrict__ xa, const u16* __restrict__ wa, const u16* __restrict__ woa,
                                                    const u16* __restrict__ xb, const u16* __restrict__ wb, const u16* __restrict__ wob,
                                                    u16* __restrict__ out, char* __restrict__ ws) {
    extern __shared__ __attribute__((aligned(16))) char ldsg[];
    lds_ptr lds = (lds_ptr)ldsg; const unsigned lds0 = (unsigned)(uintptr_t)ldsg;
    const int tid = threadIdx.x, lane = tid & 63; const int wid = rfl(tid >> 6);
    const int g = lane >> 4, r16 = lane & 15;
    const int xcd = blockIdx.x & 7, idx = blockIdx.x >> 3;
    const __amdgpu_buffer_rsrc_t wsr = __builtin_amdgcn_make_buffer_rsrc(ws, 0, WS_END, 0x00020000);
    u64 ep; asm volatile("global_load_dwordx2 %0, %1, %2 sc1" : "=v"(ep) : "v"(WS_FL), "s"(ws) : "memory");
    if (tid == 0) *(LDSP(unsigned))(lds + L_CTL) = 0;

    const int rb = (xcd >> 1) * 8 + (idx & 7), cg = (xcd & 1) * 4 + (idx >> 3);
    const int mi = wid >> 2, hh = wid & 3;
    const unsigned xvo = ((rb * 64 + wid * 8 + (lane >> 3)) * DM + (((lane & 7) ^ ((4 * wid + (lane >> 4)) & 7)) * 8)) * 2;
    const unsigned wvo = ((wid * 8 + (lane >> 3)) * 1536 + cg * 64 + (((lane & 7) ^ ((((lane >> 4) & 1) | ((wid & 1) << 1)) << 1)) * 8)) * 2;
    int hrow = rb * 64 - 16 + 2 * wid + (lane >> 5); hrow = hrow < 0 ? 0 : hrow;
    const unsigned hvo = (hrow * DM + ((((lane & 31) >> 2) ^ wid) * 8) + (lane & 3) * 2) * 2;
    const unsigned dw = lds0 + wid * 1024;
#define PIECE(t, p) do { constexpr int s_ = (t) & 7; const unsigned sl_ = ((t) & 3) * STAGE; \
        if ((p) == 0) dma16<s_ * 128>((t) < 8 ? xb : xa, xvo, dw + sl_ + OFF_X); \
        else if ((p) <= 3) dma16<((p) - 1) * 1024>((const char*)((t) < 8 ? wb : wa) + (size_t)s_ * 196608, wvo, dw + sl_ + OFF_W + ((p) - 1) * 8192); \
        else if ((t) >= 8) dma4<s_ * 128>(xa, hvo, lds0 + sl_ + OFF_H + wid * 256); } while (0)
#define PIECES(t) do { PIECE(t, 0); PIECE(t, 1); PIECE(t, 2); PIECE(t, 3); PIECE(t, 4); } while (0)
    PIECES(0); PIECES(1); PIECES(2);
    const int xo = OFF_X + mi * 4096 + r16 * 128 + ((g ^ (r16 >> 1)) * 16), ho = OFF_H + r16 * 128 + ((g ^ (r16 >> 1)) * 16);
    const int wo_ = OFF_W + (8 * g + (r16 >> 2)) * 128 + ((hh ^ (((r16 >> 3) & 1) | ((g & 1) << 1))) * 32) + (lane & 3) * 8;
    f32x4 aq0 = {}, aq1 = {}, ak0 = {}, ak1 = {}, av0 = {}, av1 = {}, ah = {};
#define ISS(NX, p) do { if ((NX) >= 0) { SBAR(); PIECE((NX) < 0 ? 0 : (NX), p); SBAR(); } } while (0)
#define STAGE_BODY(t, NX, NX2) do { lds_ptr st = lds + ((t) & 3) * STAGE; constexpr bool A_ = (t) >= 8; \
        bf16x8 x0 = ldsfrag(st + xo), x1 = ldsfrag(st + xo + 2048), wq = trfrag(st + wo_, 512), wk = trfrag(st + wo_ + 8192, 512), wv = trfrag(st + wo_ + 16384, 512), xh; \
        if (A_) xh = ldsfrag(st + ho); \
        ISS(NX, 0); \
        bf16x8 y0 = ldsfrag(st + (xo ^ 64)), y1 = ldsfrag(st + (xo ^ 64) + 2048), vq = trfrag(st + wo_ + 4096, 512), vk = trfrag(st + wo_ + 12288, 512), vv = trfrag(st + wo_ + 20480, 512), yh; \
        if (A_) yh = ldsfrag(st + (ho ^ 64)); \
        ISS(NX2, 0); \
        aq0 = MFMA16(wq, x0, aq0); aq1 = MFMA16(wq, x1, aq1); ak0 = MFMA16(wk, x0, ak0); \
        ISS(NX, 1); ISS(NX2, 1); \
        ak1 = MFMA16(wk, x1, ak1); \
        if (A_) { av0 = MFMA16(wv, x0, av0); av1 = MFMA16(wv, x1, av1); ah = MFMA16(mi ? wv : wk, xh, ah); } else { av0 = MFMA16(x0, wv, av0); av1 = MFMA16(x1, wv, av1); } \
        ISS(NX, 2); ISS(NX2, 2); \
        aq0 = MFMA16(vq, y0, aq0); aq1 = MFMA16(vq, y1, aq1); ak0 = MFMA16(vk, y0, ak0); \
        ISS(NX, 3); ISS(NX2, 3); \
        ak1 = MFMA16(vk, y1, ak1); \
        if (A_) { av0 = MFMA16(vv, y0, av0); av1 = MFMA16(vv, y1, av1); ah = MFMA16(mi ? vv : vk, yh, ah); } else { av0 = MFMA16(y0, vv, av0); av1 = MFMA16(y1, vv, av1); } \
        ISS(NX, 4); ISS(NX2, 4); SBAR(); } while (0)
    asm volatile("s_waitcnt vmcnt(8) lgkmcnt(0)\n\ts_barrier" : "+v"(ep) :: "memory");
    ep = (((u64)rfl((unsigned)(ep >> 32)) << 32) | rfl((unsigned)ep)) + 1;
    STAGE_BODY(0, 3, -1); WAIT_BAR(8); STAGE_BODY(1, 4, -1); WAIT_BAR(8); STAGE_BODY(2, 5, -1); WAIT_BAR(8); STAGE_BODY(3, 6, -1); WAIT_BAR(8);
    STAGE_BODY(4, 7, -1); WAIT_BAR(8); STAGE_BODY(5, 8, -1); WAIT_BAR(9); STAGE_BODY(6, 9, -1); WAIT_BAR(10); STAGE_BODY(7, 10, -1);
    {
        const unsigned fo = ((cg * 4 + hh) * 64 + rb * 2 + mi) * 1024;
        const unsigned qo = fo + ((lane >> 4) & 1) * 512 + (r16 + 16 * (lane >> 5)) * 16;
        __builtin_amdgcn_raw_buffer_store_b128(pack_pair(aq0, aq1, QSCALE), wsr, WS_Q + qo, 0, 16);
        __builtin_amdgcn_raw_buffer_store_b128(pack_pair(ak0, ak1, 1.0f), wsr, WS_K + qo, 0, 16);
        __builtin_amdgcn_raw_buffer_store_b128(pack_pair(av0, av1, 1.0f), wsr, WS_V + fo + lane * 16, 0, 16);
        aq0 = aq1 = ak0 = ak1 = av0 = av1 = (f32x4){};
    }
    WAIT_BAR(13); STAGE_BODY(8, -1, -1);
    WAIT_BAR(0);
    if (tid == 0) __hip_atomic_store((gu64*)(ws + FL_B1 + (cg * 32 + rb) * 64), ep, RLX_AGENT);
    STAGE_BODY(9, 11, 12); WAIT_BAR(10); STAGE_BODY(10, 13, -1); WAIT_BAR(6); STAGE_BODY(11, 14, -1); WAIT_BAR(10); STAGE_BODY(12, 15, -1); WAIT_BAR(10);
    STAGE_BODY(13, -1, -1); WAIT_BAR(5);
    const int head = xcd * 4 + (idx & 3), qb = idx >> 2;
    const char* f1 = ws + FL_B1 + (xcd * 32 + (lane & 31)) * 64;
    u64 pv = 0;
    if (wid == 0) asm volatile("global_load_dwordx2 %0, %1, off sc1" : "=v"(pv) : "v"(f1) : "memory");
    STAGE_BODY(14, -1, -1);
    asm volatile("s_waitcnt vmcnt(0) lgkmcnt(0)\n\ts_barrier" : "+v"(pv) :: "memory");
    if (wid == 0 && !__all(pv == ep)) spin_until(f1, ep);
    STAGE_BODY(15, -1, -1);
    LDS_BAR();

    const int qt = qb * 8 + (wid & 3) * 2;
    const unsigned kvo = (head * 64 + (wid >> 2) * 32 + (wid & 3)) * 1024 + lane * 16;
    const unsigned kvd = lds0 + ((wid >> 2) * 32 + (wid & 3)) * 1024;
#define KV(c) do { dma16_dev<0>(ws + WS_K + (c) * 4096, kvo, kvd + (c) * 4096); dma16_dev<0>(ws + WS_V + (c) * 4096, kvo, kvd + L_V + (c) * 4096); } while (0)
    bf16x8 qa, qc;
    { const unsigned qvo = (head * 64 + qt) * 1024 + lane * 16; ld16_dev<0>(qa, ws + WS_Q, qvo); ld16_dev<1024>(qc, ws + WS_Q, qvo); }
    KV(0); KV(1);
    {
        constexpr int PL = 80 * 16;
        lds_ptr pl = lds + L_PLANES;
        auto put = [&](int type, int row, f32x4 a, float sc) {
            u32x2 v = {cvtpk(a[0] * sc, a[1] * sc), cvtpk(a[2] * sc, a[3] * sc)};
            *(LDSP(u32x2))(pl + ((type * 4 + hh) * 2 + (g & 1)) * PL + row * 16 + (g >> 1) * 8) = v; };
        put(0, 16 + 32 * mi + r16, aq0, QSCALE); put(0, 32 + 32 * mi + r16, aq1, QSCALE);
        put(1, 16 + 32 * mi + r16, ak0, 1.0f);   put(1, 32 + 32 * mi + r16, ak1, 1.0f);
        put(2, 16 + 32 * mi + r16, av0, 1.0f);   put(2, 32 + 32 * mi + r16, av1, 1.0f);
        put(1 + mi, r16, ah, 1.0f);
        KV(2); KV(3);
        LDS_BAR();
        const int h = lane >> 5, tk = 32 * mi + (lane & 31), pos = rb * 64 + tk;
        lds_ptr qp = pl + (hh * 2 + h) * PL + (16 + tk) * 16;
        const u32x4 qv = *(LDSP(const u32x4))qp;
        float lg[8]; float m = -INFINITY;
#pragma unroll
        for (int w = 0; w < 8; ++w) {
            const u32x4 kv = *(LDSP(const u32x4))(qp + 8 * PL - w * 32);
            float d = 0.f;
#pragma unroll
            for (int i = 0; i < 4; ++i) { const unsigned a_ = qv[i], b_ = kv[i]; d = __builtin_amdgcn_fdot2_f32_bf16(__builtin_bit_cast(bf2_t, a_), __builtin_bit_cast(bf2_t, b_), d, false); }
            auto rr = __builtin_amdgcn_permlane32_swap(__float_as_uint(d), __float_as_uint(d), false, false);
            d = __uint_as_float(rr[0]) + __uint_as_float(rr[1]);
            lg[w] = (pos - 2 * w >= 0) ? d : -INFINITY;
            m = fmaxf(m, lg[w]);
        }
        SBAR(); KV(4); KV(5); SBAR();
        float l = 0.f, o[8] = {};
#pragma unroll
        for (int w = 0; w < 8; ++w) {
            const float p = __builtin_amdgcn_exp2f(lg[w] - m); l += p;
            const u32x4 vv = *(LDSP(const u32x4))(qp + 16 * PL - w * 32);
#pragma unroll
            for (int i = 0; i < 4; ++i) { o[2 * i] += p * __uint_as_float(vv[i] << 16); o[2 * i + 1] += p * __uint_as_float(vv[i] & 0xffff0000u); }
        }
        SBAR(); KV(6); KV(7); SBAR();
        const float rl = __builtin_amdgcn_rcpf(l);
        u32x4 ov = {cvtpk(o[0] * rl, o[1] * rl), cvtpk(o[2] * rl, o[3] * rl), cvtpk(o[4] * rl, o[5] * rl), cvtpk(o[6] * rl, o[7] * rl)};
        __builtin_amdgcn_raw_buffer_store_b128(ov, wsr, WS_OA + ((rb * 2 + mi) * 32 + cg * 4 + hh) * 1024 + lane * 16, 0, 16);
    }
    asm volatile("s_waitcnt vmcnt(15) lgkmcnt(0)\n\ts_barrier" : "+v"(qa), "+v"(qc) :: "memory");

    const int orb = xcd * 4 + (idx & 3), ocb = idx >> 2;
    const char* fA = ws + FL_A + (orb * 8 + (lane & 7)) * 64;
    f32x16 oa, ob;
    {
        const bf16x8 k0 = ldsfrag(lds + lane * 16);
        float ma = halfmax(rowmax16(MFMA32(k0, qa, (f32x16){}))), mb = halfmax(rowmax16(MFMA32(k0, qc, (f32x16){})));
        const unsigned ka = lds0 + (wid >> 2) * 32768 + lane * 16, va = lds0 + L_V + (wid >> 2) * 32768 + (lane >> 5) * 256 + r16 * 16;
        const unsigned ones = (lane & 31) == 16 ? 0x3f803f80u : 0u;
        pv = 0;
        for (int pass = 0;; ++pass) {
            oa = (f32x16){}; ob = (f32x16){};
            attn_keyloop(oa, ob, qa, qc, splat16(-ma), splat16(-mb), ka, va, ones, rfl(pass ? -1 : 0), rfl(pass ? -1 : 6), wid, ws + FL_A + (rb * 8 + cg) * 64, ep, fA, pv);
            bool ok = oa[8] < 1.8446744e19f && ob[8] < 1.8446744e19f;
#pragma unroll
            for (int i = 0; i < 8; ++i) ok = ok && fabsf(oa[i]) <= 1.0e38f && fabsf(ob[i]) <= 1.0e38f;
            const unsigned bad = __all(ok) ? 0u : 1u;
            if (lane == 0) *(LDSP(unsigned))(lds + L_CTL + 16 + pass * 32 + wid * 4) = bad;
            if (wid >= 4) {
#pragma unroll
                for (int i = 0; i < 9; ++i) { *(LDSP(float))(lds + L_COMB + ((wid & 3) * 18 + i) * 256 + lane * 4) = oa[i]; *(LDSP(float))(lds + L_COMB + ((wid & 3) * 18 + 9 + i) * 256 + lane * 4) = ob[i]; }
            }
            LDS_BAR();
            const u32x4 b0 = *(LDSP(const u32x4))(lds + L_CTL + 16 + pass * 32), b1 = *(LDSP(const u32x4))(lds + L_CTL + 32 + pass * 32);
            const unsigned anybad = rfl(b0[0] | b0[1] | b0[2] | b0[3] | b1[0] | b1[1] | b1[2] | b1[3]);
            if (__builtin_expect(anybad == 0 || pass == 1, 1)) break;
            ma = mb = -INFINITY;
#pragma unroll 1
            for (int t = 0; t < 64; ++t) { const bf16x8 kf = ldsfrag(lds + t * 1024 + lane * 16);
                ma = fmaxf(ma, rowmax16(MFMA32(kf, qa, (f32x16){}))); mb = fmaxf(mb, rowmax16(MFMA32(kf, qc, (f32x16){}))); }
            ma = halfmax(ma); mb = halfmax(mb);
        }
    }
    const unsigned wvo2 = (((wid & 3) * 8 + (lane >> 3)) * DM + ocb * 64 + (((lane & 7) ^ (((lane >> 4) & 3) << 1)) * 8)) * 2;
    if (wid < 4) {
        auto fin = [&](f32x16 o, int which) {
#pragma unroll
            for (int i = 0; i < 9; ++i) o[i] += *(LDSP(const float))(lds + L_COMB + (wid * 18 + which * 9 + i) * 256 + lane * 4);
            auto rr = __builtin_amdgcn_permlane32_swap(__float_as_uint(o[8]), __float_as_uint(o[8]), false, false);
            const float rl = __builtin_amdgcn_rcpf(__uint_as_float(rr[0]));
            u32x4 ov = {cvtpk(o[0] * rl, o[1] * rl), cvtpk(o[2] * rl, o[3] * rl), cvtpk(o[4] * rl, o[5] * rl), cvtpk(o[6] * rl, o[7] * rl)};
            __builtin_amdgcn_raw_buffer_store_b128(ov, wsr, WS_OB + ((qt + which) * 32 + head) * 1024 + lane * 16, 0, 16); };
        fin(oa, 0); fin(ob, 1);
        asm volatile("s_waitcnt vmcnt(0)" ::: "memory");
        if (lane == 0) { const unsigned n = __hip_atomic_fetch_add((LDSP(unsigned))(lds + L_CTL), 1u, __ATOMIC_RELAXED, __HIP_MEMORY_SCOPE_WORKGROUP);
            if (n == 3) __hip_atomic_store((gu64*)(ws + FL_B2 + (qb * 32 + head) * 64), ep, RLX_AGENT); }
    } else {
#pragma unroll
        for (int i = 0; i < 16; ++i) dma16<0>((const char*)woa + i * 32768, wvo2, lds0 + i * 4096 + (wid & 3) * 1024);
    }
    if (!__all(pv == ep)) spin_until(fA, ep);
    const int rg = wid & 3, kh = wid >> 2;
    const unsigned ovo = ((orb * 2 + (rg >> 1)) * 32 + kh * 16 + (g >> 1)) * 1024 + (g & 1) * 512 + ((rg & 1) * 16 + r16) * 16;
    bf16x8 of[8];
#define OLOAD(base) do { ld16_dev<0>(of[0], base, ovo); ld16_dev<2048>(of[1], base, ovo); ld16_dev<0>(of[2], base + 4096, ovo); ld16_dev<2048>(of[3], base + 4096, ovo); \
        ld16_dev<0>(of[4], base + 8192, ovo); ld16_dev<2048>(of[5], base + 8192, ovo); ld16_dev<0>(of[6], base + 12288, ovo); ld16_dev<2048>(of[7], base + 12288, ovo); } while (0)
#define OPIN "+v"(of[0]), "+v"(of[1]), "+v"(of[2]), "+v"(of[3]), "+v"(of[4]), "+v"(of[5]), "+v"(of[6]), "+v"(of[7])
    OLOAD(ws + WS_OA);
    if (wid >= 4) {
#pragma unroll
        for (int i = 0; i < 16; ++i) dma16<0>((const char*)wob + i * 32768, wvo2, lds0 + 65536 + i * 4096 + (wid & 3) * 1024);
    }
    asm volatile("s_cmp_lt_u32 %8, 4\n\ts_cbranch_scc1 W0%=\n\ts_waitcnt vmcnt(16)\n\ts_branch W1%=\n\tW0%=:\n\ts_waitcnt vmcnt(0)\n\tW1%=:\n\ts_waitcnt lgkmcnt(0)\n\ts_barrier"
                 : OPIN : "s"(wid) : "memory", "scc");
    const int wro = (kh * 256 + 16 * (g >> 1) + 4 * (g & 1) + (r16 >> 2)) * 128 + (lane & 3) * 8, phi = ((g & 1) << 1) | (r16 >> 3);
    const int rdo = L_RED + kh * 16384 + (rg * 16 + r16) * 256, orow = tid >> 3, och = tid & 7, c0 = L_RED + orow * 256 + (((2 * och) ^ (orow & 7)) * 16);
    u16* op = out + (size_t)(orb * 64 + orow) * 1024 + ocb * 64 + och * 8;
    auto reduce_store = [&](f32x4 (&acc)[4], int br, bool drain) {
#pragma unroll
        for (int nb = 0; nb < 4; ++nb) *(LDSP(f32x4))(lds + rdo + (((nb * 4 + g) ^ (r16 & 7)) * 16)) = acc[nb];
        if (drain) WAIT_BAR(0); else LDS_BAR();
        f32x4 a0 = *(LDSP(const f32x4))(lds + c0), a1 = *(LDSP(const f32x4))(lds + (c0 ^ 16)), b0 = *(LDSP(const f32x4))(lds + 16384 + c0), b1 = *(LDSP(const f32x4))(lds + 16384 + (c0 ^ 16));
        a0 += b0; a1 += b1;
        *(u32x4*)(op + br * 512) = (u32x4){cvtpk(a0[0], a0[1]), cvtpk(a0[2], a0[3]), cvtpk(a1[0], a1[1]), cvtpk(a1[2], a1[3])}; };
    bf16x8 wf[8][4];
    {
        f32x4 acc[4] = {};
#pragma unroll
        for (int s = 0; s < 8; ++s)
#pragma unroll
            for (int nb = 0; nb < 4; ++nb) wf[s][nb] = trfrag(lds + wro + s * 4096 + ((nb ^ phi) * 32), 1024);
#pragma unroll
        for (int s = 0; s < 8; ++s)
#pragma unroll
            for (int nb = 0; nb < 4; ++nb) acc[nb] = MFMA16(wf[s][nb], of[s], acc[nb]);
        reduce_store(acc, 0, true);
    }
#pragma unroll
    for (int s = 0; s < 8; ++s)
#pragma unroll
        for (int nb = 0; nb < 4; ++nb) wf[s][nb] = trfrag(lds + 65536 + wro + s * 4096 + ((nb ^ phi) * 32), 1024);
    if (wid == 0) {
        spin_until(ws + FL_B2 + ((orb >> 2) * 32 + (lane & 31)) * 64, ep);
        if (blockIdx.x == 0 && lane == 0) __hip_atomic_store((gu64*)(ws + WS_FL), ep, RLX_AGENT);
    }
    LDS_BAR();
    OLOAD(ws + WS_OB);
    {
        f32x4 acc[4] = {};
#define OSTEP(s, N) do { asm volatile("s_waitcnt vmcnt(" #N ")" : "+v"(of[s]) :: "memory"); \
        acc[0] = MFMA16(wf[s][0], of[s], acc[0]); acc[1] = MFMA16(wf[s][1], of[s], acc[1]); acc[2] = MFMA16(wf[s][2], of[s], acc[2]); acc[3] = MFMA16(wf[s][3], of[s], acc[3]); SBAR(); } while (0)
        OSTEP(0, 7); OSTEP(1, 6); OSTEP(2, 5); OSTEP(3, 4); OSTEP(4, 3); OSTEP(5, 2); OSTEP(6, 1); OSTEP(7, 0);
        reduce_store(acc, 1, false);
    }
}

extern "C" void kernel_launch(void* const* d_in, const int* in_sizes, int n_in, void* d_out, int out_size, void* d_ws, size_t ws_size, hipStream_t stream) {
    (void)hipFuncSetAttribute((const void*)fused_mha, hipFuncAttributeMaxDynamicSharedMemorySize, LDS_BYTES);
    fused_mha<<<256, 512, LDS_BYTES, stream>>>((const u16*)d_in[0], (const u16*)d_in[1], (const u16*)d_in[2], (const u16*)d_in[3], (const u16*)d_in[4], (const u16*)d_in[5],
                                               (u16*)d_out, (char*)d_ws);
}
```

```cpp
#include <hip/hip_runtime.h>
#include <hip/hip_bf16.h>
#include <stdint.h>

typedef short bf16x8 __attribute__((ext_vector_type(8)));
typedef short s16x4 __attribute__((ext_vector_type(4)));
typedef float f32x2 __attribute__((ext_vector_type(2)));
typedef float f32x4 __attribute__((ext_vector_type(4)));
typedef float f32x16 __attribute__((ext_vector_type(16)));
typedef unsigned u32x4 __attribute__((ext_vector_type(4)));
typedef unsigned u32x2 __attribute__((ext_vector_type(2)));
typedef __bf16 bf2_t __attribute__((ext_vector_type(2)));
typedef unsigned short u16;
typedef unsigned long long u64;
#define LDSP(T) __attribute__((address_space(3))) T*
typedef LDSP(char) lds_ptr;
typedef __attribute__((address_space(1))) u64 gu64;

constexpr int DM = 512;
constexpr float QSCALE = 0.25f * 1.4426950408889634f;
constexpr unsigned WS_Q = 0, WS_K = 2u << 20, WS_V = 4u << 20, WS_OA = 6u << 20, WS_OB = 8u << 20, WS_FL = 10u << 20, WS_END = WS_FL + 65536;
constexpr unsigned FL_B1 = WS_FL + 4096, FL_A = FL_B1 + 16384, FL_B2 = FL_A + 16384, FL_H = FL_B1 + 8192;
constexpr int STAGE = 43008, OFF_X = 24576, OFF_H = 40960;
constexpr int L_V = 65536, L_COMB = 131072, L_Q = L_COMB + 4096, L_MASK = L_Q + 8192, L_ONE = L_COMB + 18432, L_CTL = 163840 - 64, LDS_BYTES = 163840;


#define MFMA16(a, b, c) __builtin_amdgcn_mfma_f32_16x16x32_bf16(a, b, c, 0, 0, 0)
#define MFMA16K16(a, b, c) __builtin_amdgcn_mfma_f32_16x16x16bf16_1k(a, b, c, 0, 0, 0)
#define MFMA32(a, b, c) __builtin_amdgcn_mfma_f32_32x32x16_bf16(a, b, c, 0, 0, 0)
#define LDS_BAR() asm volatile("s_waitcnt lgkmcnt(0)\n\ts_barrier" ::: "memory")
#define SBAR() __builtin_amdgcn_sched_barrier(0)
#define RLX_AGENT __ATOMIC_RELAXED, __HIP_MEMORY_SCOPE_AGENT

__device__ __forceinline__ unsigned cvtpk(float lo, float hi) { return __builtin_bit_cast(unsigned, __builtin_convertvector((f32x2){lo, hi}, bf2_t)); }
template <int IMM> __device__ __forceinline__ void dma16(const void* base, unsigned voff, unsigned dst) {
    asm volatile("s_mov_b32 m0, %2\n\ts_nop 0\n\tglobal_load_lds_dwordx4 %0, %1 offset:%3" :: "v"(voff), "s"(base), "s"(dst - IMM), "n"(IMM) : "memory"); }
template <int IMM> __device__ __forceinline__ void dma16_dev(const void* base, unsigned voff, unsigned dst) {
    asm volatile("s_mov_b32 m0, %2\n\ts_nop 0\n\tglobal_load_lds_dwordx4 %0, %1 offset:%3 sc1" :: "v"(voff), "s"(base), "s"(dst - IMM), "n"(IMM) : "memory"); }
template <int IMM> __device__ __forceinline__ void dma4(const void* base, unsigned voff, unsigned dst) {
    asm volatile("s_mov_b32 m0, %2\n\ts_nop 0\n\tglobal_load_lds_dword %0, %1 offset:%3" :: "v"(voff), "s"(base), "s"(dst - IMM), "n"(IMM) : "memory"); }
template <int IMM> __device__ __forceinline__ void ld16_dev(bf16x8& r, const void* base, unsigned voff) {
    asm volatile("global_load_dwordx4 %0, %1, %2 offset:%3 sc1" : "=v"(r) : "v"(voff), "s"(base), "n"(IMM) : "memory"); }
typedef short v4i16_t __attribute__((ext_vector_type(4)));
__device__ __forceinline__ s16x4 trread(lds_ptr p) { return __builtin_bit_cast(s16x4, __builtin_amdgcn_ds_read_tr16_b64_v4i16((LDSP(v4i16_t))p)); }
__device__ __forceinline__ bf16x8 trfrag(lds_ptr p, int second) {
    s16x4 a = trread(p), b = trread(p + second);
    return (bf16x8){a[0], a[1], a[2], a[3], b[0], b[1], b[2], b[3]}; }
__device__ __forceinline__ bf16x8 ldsfrag(lds_ptr p) { return *(LDSP(const bf16x8))p; }
__device__ __forceinline__ unsigned rfl(unsigned x) { return __builtin_amdgcn_readfirstlane(x); }
template <int NAP = 4> __device__ __forceinline__ void spin_until(const char* word, u64 ep) {
    const u64 t0 = __builtin_amdgcn_s_memrealtime();
    while (!__all(__hip_atomic_load((gu64*)word, RLX_AGENT) == ep)) {
        __builtin_amdgcn_s_sleep(NAP);
        if (__builtin_amdgcn_s_memrealtime() - t0 > 5000000ull) break;
    }
    asm volatile("" ::: "memory");
}
__device__ __forceinline__ u32x4 pack_pair(f32x4 a, f32x4 b, float s, float t) {
    unsigned a0 = cvtpk(a[0] * s, a[1] * s), a1 = cvtpk(a[2] * s, a[3] * s), b0 = cvtpk(b[0] * t, b[1] * t), b1 = cvtpk(b[2] * t, b[3] * t);
    auto r0 = __builtin_amdgcn_permlane32_swap(a0, b0, false, false);
    auto r1 = __builtin_amdgcn_permlane32_swap(a1, b1, false, false);
    return (u32x4){r0[0], r1[0], r0[1], r1[1]};
}
__device__ __forceinline__ u32x4 pack_cols(f32x4 a, f32x4 b) {
    auto r0 = __builtin_amdgcn_permlane16_swap(cvtpk(a[0], a[1]), cvtpk(b[0], b[1]), false, false);
    auto r1 = __builtin_amdgcn_permlane16_swap(cvtpk(a[2], a[3]), cvtpk(b[2], b[3]), false, false);
    return (u32x4){r0[0], r1[0], r0[1], r1[1]};
}
__device__ __forceinline__ float max2(float a, float b) { return __builtin_elementwise_maximum(a, b); }
__device__ __forceinline__ float max3(float a, float b, float c) { return max2(max2(a, b), c); }
__device__ __forceinline__ float rowmax16(const f32x16& s) {
    return max2(max3(max3(max3(s[0], s[1], s[2]), s[6], s[7]), max3(s[10], s[11], s[14]), s[15]), max3(max3(max3(s[3], s[4], s[5]), s[8], s[9]), s[12], s[13]));
}
__device__ __forceinline__ float halfmax(float m) {
    auto rr = __builtin_amdgcn_permlane32_swap(__float_as_uint(m), __float_as_uint(m), false, false);
    return max2(__uint_as_float(rr[0]), __uint_as_float(rr[1]));
}
__device__ __forceinline__ s16x4 cvt4(f32x4 a, float s) { return __builtin_bit_cast(s16x4, (u32x2){cvtpk(a[0] * s, a[1] * s), cvtpk(a[2] * s, a[3] * s)}); }
template <int OFF> __device__ __forceinline__ void lds_arrive(unsigned c, unsigned one) {
    asm volatile("s_mov_b64 exec, 1\n\tds_add_u32 %0, %1 offset:%2\n\ts_mov_b64 exec, -1" :: "v"(c), "v"(one), "n"(OFF) : "memory"); }
template <int OFF> __device__ __forceinline__ unsigned lds_arrive_rtn(unsigned c, unsigned one) {
    unsigned r, n;
    asm volatile("s_mov_b64 exec, 1\n\tds_add_rtn_u32 %0, %2, %3 offset:%4\n\ts_waitcnt lgkmcnt(0)\n\tv_readfirstlane_b32 %1, %0\n\ts_mov_b64 exec, -1" : "=&v"(r), "=s"(n) : "v"(c), "v"(one), "n"(OFF) : "memory");
    return n; }
template <int OFF, int N> __device__ __forceinline__ void lds_wait(unsigned c) {
    unsigned r, n, i;
    asm volatile("s_mov_b32 %2, 0x400000\n\tA%=:\n\tds_read_b32 %0, %3 offset:%4\n\ts_waitcnt lgkmcnt(0)\n\tv_readfirstlane_b32 %1, %0\n\ts_cmp_ge_u32 %1, %5\n\ts_cbranch_scc1 B%=\n\t"
                 "s_sleep 1\n\ts_sub_u32 %2, %2, 1\n\ts_cbranch_scc0 A%=\n\tB%=:" : "=&v"(r), "=&s"(n), "=&s"(i) : "v"(c), "n"(OFF), "n"(N) : "memory", "scc"); }
__device__ __forceinline__ f32x16 splat16(float v) { f32x16 r;
#pragma unroll
    for (int i = 0; i < 16; ++i) r[i] = v;
    return r; }

__device__ __forceinline__ void attn_keyloop(f32x16& oa, f32x16& ob, bf16x8 qa, bf16x8 qb, float ma, float mb, unsigned ka, unsigned va, unsigned vinc,
                                             unsigned kvo, const void* kb, const void* vb, unsigned kd, int pub, const void* fa, unsigned long long ep, const void* pf) {
    int it; unsigned pw;
    asm volatile(
        "ds_read_b128 v[144:147], %[ka] offset:0\n\tds_read_b128 v[172:175], %[va] offset:512\n\tds_read_b128 v[164:167], %[ka] offset:1024\n\t"
        "ds_read_b128 v[148:151], %[va] offset:0\n\tds_read_b128 v[152:155], %[va] offset:512\n\tv_xor_b32 v104, 0x80000000, %[ma]\n\t"
        "v_xor_b32 v105, 0x80000000, %[ma]\n\tv_mov_b64 v[106:107], v[104:105]\n\tv_mov_b64 v[108:109], v[104:105]\n\tv_mov_b64 v[110:111], v[104:105]\n\t"
        "v_mov_b64 v[112:113], v[104:105]\n\tv_mov_b64 v[114:115], v[104:105]\n\tv_mov_b64 v[116:117], v[104:105]\n\tv_mov_b64 v[118:119], v[104:105]\n\t"
        "v_xor_b32 v120, 0x80000000, %[mb]\n\tv_xor_b32 v121, 0x80000000, %[mb]\n\ts_mov_b32 %[it], 1\n\ts_waitcnt lgkmcnt(3)\n\t"
        "v_mfma_f32_32x32x16_bf16 v[40:55], v[144:147], v[136:139], v[104:119]\n\tv_mov_b64 v[122:123], v[120:121]\n\tv_mov_b64 v[124:125], v[120:121]\n\t"
        "v_mov_b64 v[126:127], v[120:121]\n\tv_mov_b64 v[128:129], v[120:121]\n\tv_mov_b64 v[130:131], v[120:121]\n\tv_mov_b64 v[132:133], v[120:121]\n\t"
        "v_mov_b64 v[134:135], v[120:121]\n\tv_mov_b64 v[160:161], 0\n\tv_mov_b32 v162, 0\n\tv_mov_b64 v[70:71], 0\n\tv_mov_b64 v[72:73], 0\n\t"
        "v_mov_b64 v[74:75], 0\n\tv_mov_b64 v[76:77], 0\n\tv_mov_b64 v[78:79], 0\n\tv_mov_b64 v[80:81], 0\n\tv_mov_b64 v[82:83], 0\n\tv_mov_b64 v[84:85], 0\n\t"
        "v_mov_b64 v[86:87], 0\n\tv_mov_b64 v[88:89], 0\n\tv_mov_b64 v[90:91], 0\n\tv_mov_b64 v[92:93], 0\n\tv_mov_b64 v[94:95], 0\n\tv_mov_b64 v[96:97], 0\n\t"
        "v_mov_b64 v[98:99], 0\n\tv_mov_b64 v[100:101], 0\n\tv_mov_b64 v[102:103], 0\n\tv_exp_f32 v40, v40\n\tv_cvt_pk_bf16_f32 v163, v70, v71\n\t"
        "s_add_u32 m0, %[kd], 8192\n\tv_add_u32 %[kvo], 4096, %[kvo]\n\tv_mfma_f32_32x32x16_bf16 v[56:71], v[144:147], v[140:143], v[120:135]\n\t"
        "s_waitcnt lgkmcnt(0)\n\tglobal_load_lds_dwordx4 %[kvo], %[kb] sc1\n\tv_exp_f32 v41, v41\n\tv_exp_f32 v42, v42\n\tv_exp_f32 v43, v43\n\t"
        "v_cvt_pk_bf16_f32 v156, v40, v41\n\tv_exp_f32 v44, v44\n\tv_exp_f32 v45, v45\n\tv_cvt_pk_bf16_f32 v157, v42, v43\n\ts_add_u32 m0, %[kd], 73728\n\t"
        "v_mfma_f32_32x32x16_bf16 v[88:103], v[172:175], v[160:163], v[88:103]\n\tds_read_b128 v[172:175], %[va] offset:1536\n\t"
        "global_load_lds_dwordx4 %[kvo], %[vb] sc1\n\tv_exp_f32 v46, v46\n\tv_exp_f32 v47, v47\n\tv_cvt_pk_bf16_f32 v158, v44, v45\n\tv_exp_f32 v48, v48\n\t"
        "v_cvt_pk_bf16_f32 v159, v46, v47\n\tv_exp_f32 v49, v49\n\tv_exp_f32 v50, v50\n\tv_exp_f32 v51, v51\n\ts_add_u32 m0, %[kd], 12288\n\t"
        "v_add_u32 %[kvo], 4096, %[kvo]\n\tv_mfma_f32_32x32x16_bf16 v[72:87], v[148:151], v[156:159], v[72:87]\n\tds_read_b128 v[144:147], %[ka] offset:2048\n\t"
        "global_load_lds_dwordx4 %[kvo], %[kb] sc1\n\tv_cvt_pk_bf16_f32 v160, v48, v49\n\tv_exp_f32 v52, v52\n\tv_exp_f32 v53, v53\n\t"
        "v_cvt_pk_bf16_f32 v161, v50, v51\n\tv_exp_f32 v54, v54\n\tv_exp_f32 v55, v55\n\tv_cvt_pk_bf16_f32 v162, v52, v53\n\tv_exp_f32 v56, v56\n\t"
        "v_cvt_pk_bf16_f32 v163, v54, v55\n\ts_add_u32 m0, %[kd], 77824\n\tv_mfma_f32_32x32x16_bf16 v[40:55], v[164:167], v[136:139], v[104:119]\n\t"
        "ds_read_b128 v[168:171], %[va] offset:1024\n\tglobal_load_lds_dwordx4 %[kvo], %[vb] sc1\n\tv_exp_f32 v57, v57\n\tv_exp_f32 v58, v58\n\t"
        "v_exp_f32 v59, v59\n\tv_cvt_pk_bf16_f32 v156, v56, v57\n\tv_exp_f32 v60, v60\n\tv_exp_f32 v61, v61\n\tv_cvt_pk_bf16_f32 v157, v58, v59\n\t"
        "s_add_u32 m0, %[kd], 16384\n\tv_add_u32 %[kvo], 4096, %[kvo]\n\tv_mfma_f32_32x32x16_bf16 v[72:87], v[152:155], v[160:163], v[72:87]\n\t"
        "global_load_lds_dwordx4 %[kvo], %[kb] sc1\n\tv_exp_f32 v62, v62\n\tv_exp_f32 v63, v63\n\tv_cvt_pk_bf16_f32 v158, v60, v61\n\tv_exp_f32 v64, v64\n\t"
        "v_cvt_pk_bf16_f32 v159, v62, v63\n\tv_exp_f32 v65, v65\n\tv_exp_f32 v66, v66\n\tv_exp_f32 v67, v67\n\ts_add_u32 m0, %[kd], 81920\n\t"
        "v_mfma_f32_32x32x16_bf16 v[88:103], v[148:151], v[156:159], v[88:103]\n\tglobal_load_lds_dwordx4 %[kvo], %[vb] sc1\n\tv_cvt_pk_bf16_f32 v160, v64, v65\n\t"
        "v_exp_f32 v68, v68\n\tv_exp_f32 v69, v69\n\tv_cvt_pk_bf16_f32 v161, v66, v67\n\tv_exp_f32 v70, v70\n\tv_exp_f32 v71, v71\n\t"
        "v_cvt_pk_bf16_f32 v162, v68, v69\n\tv_exp_f32 v40, v40\n\tv_cvt_pk_bf16_f32 v163, v70, v71\n\ts_add_u32 m0, %[kd], 20480\n\t"
        "v_add_u32 %[kvo], 4096, %[kvo]\n\tv_mfma_f32_32x32x16_bf16 v[56:71], v[164:167], v[140:143], v[120:135]\n\ts_waitcnt lgkmcnt(0)\n\t"
        "global_load_lds_dwordx4 %[kvo], %[kb] sc1\n\tv_exp_f32 v41, v41\n\tv_exp_f32 v42, v42\n\tv_exp_f32 v43, v43\n\tv_cvt_pk_bf16_f32 v156, v40, v41\n\t"
        "v_exp_f32 v44, v44\n\tv_exp_f32 v45, v45\n\tv_cvt_pk_bf16_f32 v157, v42, v43\n\ts_add_u32 m0, %[kd], 86016\n\t"
        "v_mfma_f32_32x32x16_bf16 v[88:103], v[152:155], v[160:163], v[88:103]\n\tds_read_b128 v[152:155], %[va] offset:2560\n\t"
        "global_load_lds_dwordx4 %[kvo], %[vb] sc1\n\tv_exp_f32 v46, v46\n\tv_exp_f32 v47, v47\n\tv_cvt_pk_bf16_f32 v158, v44, v45\n\tv_exp_f32 v48, v48\n\t"
        "v_cvt_pk_bf16_f32 v159, v46, v47\n\tv_exp_f32 v49, v49\n\tv_exp_f32 v50, v50\n\tv_exp_f32 v51, v51\n\ts_add_u32 m0, %[kd], 24576\n\t"
        "v_add_u32 %[kvo], 4096, %[kvo]\n\tv_mfma_f32_32x32x16_bf16 v[72:87], v[168:171], v[156:159], v[72:87]\n\tds_read_b128 v[164:167], %[ka] offset:3072\n\t"
        "global_load_lds_dwordx4 %[kvo], %[kb] sc1\n\tv_cvt_pk_bf16_f32 v160, v48, v49\n\tv_exp_f32 v52, v52\n\tv_exp_f32 v53, v53\n\t"
        "v_cvt_pk_bf16_f32 v161, v50, v51\n\tv_exp_f32 v54, v54\n\tv_exp_f32 v55, v55\n\tv_cvt_pk_bf16_f32 v162, v52, v53\n\tv_exp_f32 v56, v56\n\t"
        "v_cvt_pk_bf16_f32 v163, v54, v55\n\ts_add_u32 m0, %[kd], 90112\n\tv_mfma_f32_32x32x16_bf16 v[40:55], v[144:147], v[136:139], v[104:119]\n\t"
        "ds_read_b128 v[148:151], %[va] offset:2048\n\tglobal_load_lds_dwordx4 %[kvo], %[vb] sc1\n\tv_exp_f32 v57, v57\n\tv_exp_f32 v58, v58\n\t"
        "v_exp_f32 v59, v59\n\tv_cvt_pk_bf16_f32 v156, v56, v57\n\tv_exp_f32 v60, v60\n\tv_exp_f32 v61, v61\n\tv_cvt_pk_bf16_f32 v157, v58, v59\n\t"
        "s_add_u32 m0, %[kd], 28672\n\tv_add_u32 %[kvo], 4096, %[kvo]\n\tv_mfma_f32_32x32x16_bf16 v[72:87], v[172:175], v[160:163], v[72:87]\n\t"
        "global_load_lds_dwordx4 %[kvo], %[kb] sc1\n\tv_exp_f32 v62, v62\n\tv_exp_f32 v63, v63\n\tv_cvt_pk_bf16_f32 v158, v60, v61\n\tv_exp_f32 v64, v64\n\t"
        "v_cvt_pk_bf16_f32 v159, v62, v63\n\tv_exp_f32 v65, v65\n\tv_exp_f32 v66, v66\n\tv_exp_f32 v67, v67\n\ts_add_u32 m0, %[kd], 94208\n\t"
        "v_mfma_f32_32x32x16_bf16 v[88:103], v[168:171], v[156:159], v[88:103]\n\tglobal_load_lds_dwordx4 %[kvo], %[vb] sc1\n\tv_cvt_pk_bf16_f32 v160, v64, v65\n\t"
        "v_exp_f32 v68, v68\n\tv_exp_f32 v69, v69\n\tv_cvt_pk_bf16_f32 v161, v66, v67\n\tv_exp_f32 v70, v70\n\tv_exp_f32 v71, v71\n\t"
        "v_cvt_pk_bf16_f32 v162, v68, v69\n\tv_exp_f32 v40, v40\n\tv_cvt_pk_bf16_f32 v163, v70, v71\n\t"
        "v_mfma_f32_32x32x16_bf16 v[56:71], v[144:147], v[140:143], v[120:135]\n\ts_waitcnt lgkmcnt(0)\n\tv_exp_f32 v41, v41\n\tv_exp_f32 v42, v42\n\t"
        "v_exp_f32 v43, v43\n\tv_cvt_pk_bf16_f32 v156, v40, v41\n\tv_exp_f32 v44, v44\n\tv_exp_f32 v45, v45\n\tv_cvt_pk_bf16_f32 v157, v42, v43\n\t"
        "s_waitcnt vmcnt(12)\n\ts_barrier\n\tv_mfma_f32_32x32x16_bf16 v[88:103], v[172:175], v[160:163], v[88:103]\n\t"
        "ds_read_b128 v[172:175], %[va] offset:3584\n\tv_exp_f32 v46, v46\n\tv_exp_f32 v47, v47\n\tv_cvt_pk_bf16_f32 v158, v44, v45\n\tv_exp_f32 v48, v48\n\t"
        "v_cvt_pk_bf16_f32 v159, v46, v47\n\tv_exp_f32 v49, v49\n\tv_exp_f32 v50, v50\n\tv_exp_f32 v51, v51\n\t"
        "v_mfma_f32_32x32x16_bf16 v[72:87], v[148:151], v[156:159], v[72:87]\n\tds_read_b128 v[144:147], %[ka] offset:4096\n\tv_cvt_pk_bf16_f32 v160, v48, v49\n\t"
        "v_exp_f32 v52, v52\n\tv_exp_f32 v53, v53\n\tv_cvt_pk_bf16_f32 v161, v50, v51\n\tv_exp_f32 v54, v54\n\tv_exp_f32 v55, v55\n\t"
        "v_cvt_pk_bf16_f32 v162, v52, v53\n\tv_exp_f32 v56, v56\n\tv_cvt_pk_bf16_f32 v163, v54, v55\n\t"
        "v_mfma_f32_32x32x16_bf16 v[40:55], v[164:167], v[136:139], v[104:119]\n\tds_read_b128 v[168:171], %[va] offset:3072\n\tv_exp_f32 v57, v57\n\t"
        "v_exp_f32 v58, v58\n\tv_exp_f32 v59, v59\n\tv_cvt_pk_bf16_f32 v156, v56, v57\n\tv_exp_f32 v60, v60\n\tv_exp_f32 v61, v61\n\t"
        "v_cvt_pk_bf16_f32 v157, v58, v59\n\tv_mfma_f32_32x32x16_bf16 v[72:87], v[152:155], v[160:163], v[72:87]\n\tv_exp_f32 v62, v62\n\tv_exp_f32 v63, v63\n\t"
        "v_cvt_pk_bf16_f32 v158, v60, v61\n\tv_exp_f32 v64, v64\n\tv_cvt_pk_bf16_f32 v159, v62, v63\n\tv_exp_f32 v65, v65\n\tv_exp_f32 v66, v66\n\t"
        "v_exp_f32 v67, v67\n\tv_mfma_f32_32x32x16_bf16 v[88:103], v[148:151], v[156:159], v[88:103]\n\tv_cvt_pk_bf16_f32 v160, v64, v65\n\tv_exp_f32 v68, v68\n\t"
        "v_exp_f32 v69, v69\n\tv_cvt_pk_bf16_f32 v161, v66, v67\n\tv_exp_f32 v70, v70\n\tv_exp_f32 v71, v71\n\tv_cvt_pk_bf16_f32 v162, v68, v69\n\t"
        "v_exp_f32 v40, v40\n\tv_cvt_pk_bf16_f32 v163, v70, v71\n\tv_mfma_f32_32x32x16_bf16 v[56:71], v[164:167], v[140:143], v[120:135]\n\t"
        "s_waitcnt lgkmcnt(0)\n\tv_exp_f32 v41, v41\n\tv_exp_f32 v42, v42\n\tv_exp_f32 v43, v43\n\tv_cvt_pk_bf16_f32 v156, v40, v41\n\tv_exp_f32 v44, v44\n\t"
        "v_exp_f32 v45, v45\n\tv_cvt_pk_bf16_f32 v157, v42, v43\n\tv_mfma_f32_32x32x16_bf16 v[88:103], v[152:155], v[160:163], v[88:103]\n\t"
        "ds_read_b128 v[152:155], %[va] offset:4608\n\tv_exp_f32 v46, v46\n\tv_exp_f32 v47, v47\n\tv_cvt_pk_bf16_f32 v158, v44, v45\n\tv_exp_f32 v48, v48\n\t"
        "v_cvt_pk_bf16_f32 v159, v46, v47\n\tv_exp_f32 v49, v49\n\tv_exp_f32 v50, v50\n\tv_exp_f32 v51, v51\n\t"
        "v_mfma_f32_32x32x16_bf16 v[72:87], v[168:171], v[156:159], v[72:87]\n\tds_read_b128 v[164:167], %[ka] offset:5120\n\tv_cvt_pk_bf16_f32 v160, v48, v49\n\t"
        "v_exp_f32 v52, v52\n\tv_exp_f32 v53, v53\n\tv_cvt_pk_bf16_f32 v161, v50, v51\n\tv_exp_f32 v54, v54\n\tv_exp_f32 v55, v55\n\t"
        "v_cvt_pk_bf16_f32 v162, v52, v53\n\tv_exp_f32 v56, v56\n\tv_cvt_pk_bf16_f32 v163, v54, v55\n\t"
        "v_mfma_f32_32x32x16_bf16 v[40:55], v[144:147], v[136:139], v[104:119]\n\tds_read_b128 v[148:151], %[va] offset:4096\n\tv_exp_f32 v57, v57\n\t"
        "v_exp_f32 v58, v58\n\tv_exp_f32 v59, v59\n\tv_cvt_pk_bf16_f32 v156, v56, v57\n\tv_exp_f32 v60, v60\n\tv_exp_f32 v61, v61\n\t"
        "v_cvt_pk_bf16_f32 v157, v58, v59\n\tv_mfma_f32_32x32x16_bf16 v[72:87], v[172:175], v[160:163], v[72:87]\n\tv_exp_f32 v62, v62\n\tv_exp_f32 v63, v63\n\t"
        "v_cvt_pk_bf16_f32 v158, v60, v61\n\tv_exp_f32 v64, v64\n\tv_cvt_pk_bf16_f32 v159, v62, v63\n\tv_exp_f32 v65, v65\n\tv_exp_f32 v66, v66\n\t"
        "v_exp_f32 v67, v67\n\tv_mfma_f32_32x32x16_bf16 v[88:103], v[168:171], v[156:159], v[88:103]\n\tv_cvt_pk_bf16_f32 v160, v64, v65\n\tv_exp_f32 v68, v68\n\t"
        "v_exp_f32 v69, v69\n\tv_cvt_pk_bf16_f32 v161, v66, v67\n\tv_exp_f32 v70, v70\n\tv_exp_f32 v71, v71\n\tv_cvt_pk_bf16_f32 v162, v68, v69\n\t"
        "v_add_u32 %[ka], 4096, %[ka]\n\tv_add_u32 %[va], %[vinc], %[va]\n\ts_waitcnt vmcnt(0)\n\ts_barrier\n\tglobal_load_dword %[pw], %[pf], off\n\t"
        "s_cmp_eq_u32 %[pub], 0\n\ts_cbranch_scc1 SL%=\n\ts_mov_b64 exec, 1\n\tglobal_store_dwordx2 %[fa], %[ep], off sc1\n\ts_mov_b64 exec, -1\n\tSL%=:\n\t"
        "s_setprio 1\n\t.p2align 6\n\tLOOQ%=:\n\tv_exp_f32 v40, v40\n\tv_cvt_pk_bf16_f32 v163, v70, v71\n\t"
        "v_mfma_f32_32x32x16_bf16 v[56:71], v[144:147], v[140:143], v[120:135]\n\ts_waitcnt lgkmcnt(0)\n\tv_exp_f32 v41, v41\n\tv_exp_f32 v42, v42\n\t"
        "v_exp_f32 v43, v43\n\tv_cvt_pk_bf16_f32 v156, v40, v41\n\tv_exp_f32 v44, v44\n\tv_exp_f32 v45, v45\n\tv_cvt_pk_bf16_f32 v157, v42, v43\n\t"
        "v_mfma_f32_32x32x16_bf16 v[88:103], v[172:175], v[160:163], v[88:103]\n\tds_read_b128 v[172:175], %[va] offset:1536\n\tv_exp_f32 v46, v46\n\t"
        "v_exp_f32 v47, v47\n\tv_cvt_pk_bf16_f32 v158, v44, v45\n\tv_exp_f32 v48, v48\n\tv_cvt_pk_bf16_f32 v159, v46, v47\n\tv_exp_f32 v49, v49\n\t"
        "v_exp_f32 v50, v50\n\tv_exp_f32 v51, v51\n\tv_mfma_f32_32x32x16_bf16 v[72:87], v[148:151], v[156:159], v[72:87]\n\t"
        "ds_read_b128 v[144:147], %[ka] offset:2048\n\tv_cvt_pk_bf16_f32 v160, v48, v49\n\tv_exp_f32 v52, v52\n\tv_exp_f32 v53, v53\n\t"
        "v_cvt_pk_bf16_f32 v161, v50, v51\n\tv_exp_f32 v54, v54\n\tv_exp_f32 v55, v55\n\tv_cvt_pk_bf16_f32 v162, v52, v53\n\tv_exp_f32 v56, v56\n\t"
        "v_cvt_pk_bf16_f32 v163, v54, v55\n\tv_mfma_f32_32x32x16_bf16 v[40:55], v[164:167], v[136:139], v[104:119]\n\t"
        "ds_read_b128 v[168:171], %[va] offset:1024\n\tv_exp_f32 v57, v57\n\tv_exp_f32 v58, v58\n\tv_exp_f32 v59, v59\n\tv_cvt_pk_bf16_f32 v156, v56, v57\n\t"
        "v_exp_f32 v60, v60\n\tv_exp_f32 v61, v61\n\tv_cvt_pk_bf16_f32 v157, v58, v59\n\tv_mfma_f32_32x32x16_bf16 v[72:87], v[152:155], v[160:163], v[72:87]\n\t"
        "v_exp_f32 v62, v62\n\tv_exp_f32 v63, v63\n\tv_cvt_pk_bf16_f32 v158, v60, v61\n\tv_exp_f32 v64, v64\n\tv_cvt_pk_bf16_f32 v159, v62, v63\n\t"
        "v_exp_f32 v65, v65\n\tv_exp_f32 v66, v66\n\tv_exp_f32 v67, v67\n\tv_mfma_f32_32x32x16_bf16 v[88:103], v[148:151], v[156:159], v[88:103]\n\t"
        "v_cvt_pk_bf16_f32 v160, v64, v65\n\tv_exp_f32 v68, v68\n\tv_exp_f32 v69, v69\n\tv_cvt_pk_bf16_f32 v161, v66, v67\n\tv_exp_f32 v70, v70\n\t"
        "v_exp_f32 v71, v71\n\tv_cvt_pk_bf16_f32 v162, v68, v69\n\tv_exp_f32 v40, v40\n\tv_cvt_pk_bf16_f32 v163, v70, v71\n\t"
        "v_mfma_f32_32x32x16_bf16 v[56:71], v[164:167], v[140:143], v[120:135]\n\ts_waitcnt lgkmcnt(0)\n\tv_exp_f32 v41, v41\n\tv_exp_f32 v42, v42\n\t"
        "v_exp_f32 v43, v43\n\tv_cvt_pk_bf16_f32 v156, v40, v41\n\tv_exp_f32 v44, v44\n\tv_exp_f32 v45, v45\n\tv_cvt_pk_bf16_f32 v157, v42, v43\n\t"
        "v_mfma_f32_32x32x16_bf16 v[88:103], v[152:155], v[160:163], v[88:103]\n\tds_read_b128 v[152:155], %[va] offset:2560\n\tv_exp_f32 v46, v46\n\t"
        "v_exp_f32 v47, v47\n\tv_cvt_pk_bf16_f32 v158, v44, v45\n\tv_exp_f32 v48, v48\n\tv_cvt_pk_bf16_f32 v159, v46, v47\n\tv_exp_f32 v49, v49\n\t"
        "v_exp_f32 v50, v50\n\tv_exp_f32 v51, v51\n\tv_mfma_f32_32x32x16_bf16 v[72:87], v[168:171], v[156:159], v[72:87]\n\t"
        "ds_read_b128 v[164:167], %[ka] offset:3072\n\tv_cvt_pk_bf16_f32 v160, v48, v49\n\tv_exp_f32 v52, v52\n\tv_exp_f32 v53, v53\n\t"
        "v_cvt_pk_bf16_f32 v161, v50, v51\n\tv_exp_f32 v54, v54\n\tv_exp_f32 v55, v55\n\tv_cvt_pk_bf16_f32 v162, v52, v53\n\tv_exp_f32 v56, v56\n\t"
        "v_cvt_pk_bf16_f32 v163, v54, v55\n\tv_mfma_f32_32x32x16_bf16 v[40:55], v[144:147], v[136:139], v[104:119]\n\t"
        "ds_read_b128 v[148:151], %[va] offset:2048\n\tv_exp_f32 v57, v57\n\tv_exp_f32 v58, v58\n\tv_exp_f32 v59, v59\n\tv_cvt_pk_bf16_f32 v156, v56, v57\n\t"
        "v_exp_f32 v60, v60\n\tv_exp_f32 v61, v61\n\tv_cvt_pk_bf16_f32 v157, v58, v59\n\tv_mfma_f32_32x32x16_bf16 v[72:87], v[172:175], v[160:163], v[72:87]\n\t"
        "v_exp_f32 v62, v62\n\tv_exp_f32 v63, v63\n\tv_cvt_pk_bf16_f32 v158, v60, v61\n\tv_exp_f32 v64, v64\n\tv_cvt_pk_bf16_f32 v159, v62, v63\n\t"
        "v_exp_f32 v65, v65\n\tv_exp_f32 v66, v66\n\tv_exp_f32 v67, v67\n\tv_mfma_f32_32x32x16_bf16 v[88:103], v[168:171], v[156:159], v[88:103]\n\t"
        "v_cvt_pk_bf16_f32 v160, v64, v65\n\tv_exp_f32 v68, v68\n\tv_exp_f32 v69, v69\n\tv_cvt_pk_bf16_f32 v161, v66, v67\n\tv_exp_f32 v70, v70\n\t"
        "v_exp_f32 v71, v71\n\tv_cvt_pk_bf16_f32 v162, v68, v69\n\tv_exp_f32 v40, v40\n\tv_cvt_pk_bf16_f32 v163, v70, v71\n\t"
        "v_mfma_f32_32x32x16_bf16 v[56:71], v[144:147], v[140:143], v[120:135]\n\ts_waitcnt lgkmcnt(0)\n\tv_exp_f32 v41, v41\n\tv_exp_f32 v42, v42\n\t"
        "v_exp_f32 v43, v43\n\tv_cvt_pk_bf16_f32 v156, v40, v41\n\tv_exp_f32 v44, v44\n\tv_exp_f32 v45, v45\n\tv_cvt_pk_bf16_f32 v157, v42, v43\n\t"
        "v_mfma_f32_32x32x16_bf16 v[88:103], v[172:175], v[160:163], v[88:103]\n\tds_read_b128 v[172:175], %[va] offset:3584\n\tv_exp_f32 v46, v46\n\t"
        "v_exp_f32 v47, v47\n\tv_cvt_pk_bf16_f32 v158, v44, v45\n\tv_exp_f32 v48, v48\n\tv_cvt_pk_bf16_f32 v159, v46, v47\n\tv_exp_f32 v49, v49\n\t"
        "v_exp_f32 v50, v50\n\tv_exp_f32 v51, v51\n\tv_mfma_f32_32x32x16_bf16 v[72:87], v[148:151], v[156:159], v[72:87]\n\t"
        "ds_read_b128 v[144:147], %[ka] offset:4096\n\tv_cvt_pk_bf16_f32 v160, v48, v49\n\tv_exp_f32 v52, v52\n\tv_exp_f32 v53, v53\n\t"
        "v_cvt_pk_bf16_f32 v161, v50, v51\n\tv_exp_f32 v54, v54\n\tv_exp_f32 v55, v55\n\tv_cvt_pk_bf16_f32 v162, v52, v53\n\tv_exp_f32 v56, v56\n\t"
        "v_cvt_pk_bf16_f32 v163, v54, v55\n\tv_mfma_f32_32x32x16_bf16 v[40:55], v[164:167], v[136:139], v[104:119]\n\t"
        "ds_read_b128 v[168:171], %[va] offset:3072\n\tv_exp_f32 v57, v57\n\tv_exp_f32 v58, v58\n\tv_exp_f32 v59, v59\n\tv_cvt_pk_bf16_f32 v156, v56, v57\n\t"
        "v_exp_f32 v60, v60\n\tv_exp_f32 v61, v61\n\tv_cvt_pk_bf16_f32 v157, v58, v59\n\tv_mfma_f32_32x32x16_bf16 v[72:87], v[152:155], v[160:163], v[72:87]\n\t"
        "v_exp_f32 v62, v62\n\tv_exp_f32 v63, v63\n\tv_cvt_pk_bf16_f32 v158, v60, v61\n\tv_exp_f32 v64, v64\n\tv_cvt_pk_bf16_f32 v159, v62, v63\n\t"
        "v_exp_f32 v65, v65\n\tv_exp_f32 v66, v66\n\tv_exp_f32 v67, v67\n\tv_mfma_f32_32x32x16_bf16 v[88:103], v[148:151], v[156:159], v[88:103]\n\t"
        "v_cvt_pk_bf16_f32 v160, v64, v65\n\tv_exp_f32 v68, v68\n\tv_exp_f32 v69, v69\n\tv_cvt_pk_bf16_f32 v161, v66, v67\n\tv_exp_f32 v70, v70\n\t"
        "v_exp_f32 v71, v71\n\tv_cvt_pk_bf16_f32 v162, v68, v69\n\tv_exp_f32 v40, v40\n\tv_cvt_pk_bf16_f32 v163, v70, v71\n\t"
        "v_mfma_f32_32x32x16_bf16 v[56:71], v[164:167], v[140:143], v[120:135]\n\ts_waitcnt lgkmcnt(0)\n\tv_exp_f32 v41, v41\n\tv_exp_f32 v42, v42\n\t"
        "v_exp_f32 v43, v43\n\tv_cvt_pk_bf16_f32 v156, v40, v41\n\tv_exp_f32 v44, v44\n\tv_exp_f32 v45, v45\n\tv_cvt_pk_bf16_f32 v157, v42, v43\n\t"
        "v_mfma_f32_32x32x16_bf16 v[88:103], v[152:155], v[160:163], v[88:103]\n\tds_read_b128 v[152:155], %[va] offset:4608\n\tv_exp_f32 v46, v46\n\t"
        "v_exp_f32 v47, v47\n\tv_cvt_pk_bf16_f32 v158, v44, v45\n\tv_exp_f32 v48, v48\n\tv_cvt_pk_bf16_f32 v159, v46, v47\n\tv_exp_f32 v49, v49\n\t"
        "v_exp_f32 v50, v50\n\tv_exp_f32 v51, v51\n\tv_mfma_f32_32x32x16_bf16 v[72:87], v[168:171], v[156:159], v[72:87]\n\t"
        "ds_read_b128 v[164:167], %[ka] offset:5120\n\tv_cvt_pk_bf16_f32 v160, v48, v49\n\tv_exp_f32 v52, v52\n\tv_exp_f32 v53, v53\n\t"
        "v_cvt_pk_bf16_f32 v161, v50, v51\n\tv_exp_f32 v54, v54\n\tv_exp_f32 v55, v55\n\tv_cvt_pk_bf16_f32 v162, v52, v53\n\tv_exp_f32 v56, v56\n\t"
        "v_cvt_pk_bf16_f32 v163, v54, v55\n\tv_mfma_f32_32x32x16_bf16 v[40:55], v[144:147], v[136:139], v[104:119]\n\t"
        "ds_read_b128 v[148:151], %[va] offset:4096\n\tv_exp_f32 v57, v57\n\tv_exp_f32 v58, v58\n\tv_exp_f32 v59, v59\n\tv_cvt_pk_bf16_f32 v156, v56, v57\n\t"
        "v_exp_f32 v60, v60\n\tv_exp_f32 v61, v61\n\tv_cvt_pk_bf16_f32 v157, v58, v59\n\tv_mfma_f32_32x32x16_bf16 v[72:87], v[172:175], v[160:163], v[72:87]\n\t"
        "s_add_u32 %[it], %[it], 1\n\tv_exp_f32 v62, v62\n\tv_exp_f32 v63, v63\n\tv_cvt_pk_bf16_f32 v158, v60, v61\n\tv_exp_f32 v64, v64\n\t"
        "v_cvt_pk_bf16_f32 v159, v62, v63\n\tv_exp_f32 v65, v65\n\tv_exp_f32 v66, v66\n\tv_exp_f32 v67, v67\n\t"
        "v_mfma_f32_32x32x16_bf16 v[88:103], v[168:171], v[156:159], v[88:103]\n\ts_cmp_lt_u32 %[it], 8\n\tv_cvt_pk_bf16_f32 v160, v64, v65\n\t"
        "v_exp_f32 v68, v68\n\tv_exp_f32 v69, v69\n\tv_cvt_pk_bf16_f32 v161, v66, v67\n\tv_exp_f32 v70, v70\n\tv_exp_f32 v71, v71\n\t"
        "v_cvt_pk_bf16_f32 v162, v68, v69\n\tv_add_u32 %[ka], 4096, %[ka]\n\tv_add_u32 %[va], %[vinc], %[va]\n\ts_cbranch_scc1 LOOQ%=\n\t"
        "v_cvt_pk_bf16_f32 v163, v70, v71\n\ts_nop 1\n\tv_mfma_f32_32x32x16_bf16 v[88:103], v[172:175], v[160:163], v[88:103]\n\ts_waitcnt vmcnt(0) lgkmcnt(0)\n\t"
        "s_nop 7\n\ts_nop 7\n\t"
        : "=&{v[72:87]}"(oa), "=&{v[88:103]}"(ob), [ka] "+v"(ka), [va] "+v"(va), [it] "=&s"(it), [kvo] "+v"(kvo), [pw] "=&v"(pw)
        : "{v[136:139]}"(qa), "{v[140:143]}"(qb), [ma] "v"(ma), [mb] "v"(mb), [vinc] "v"(vinc),
          [kb] "s"(kb), [vb] "s"(vb), [kd] "s"(kd), [pub] "s"(pub), [fa] "v"(fa), [ep] "v"(ep), [pf] "v"(pf)
        : "memory", "scc", "v40", "v41", "v42", "v43", "v44", "v45", "v46", "v47", "v48", "v49", "v50", "v51", "v52", "v53", "v54", "v55", "v56", "v57", "v58", "v59", "v60", "v61", "v62", "v63", "v64", "v65", "v66", "v67", "v68", "v69", "v70", "v71", "v104", "v105", "v106", "v107", "v108", "v109", "v110", "v111", "v112", "v113", "v114", "v115", "v116", "v117", "v118", "v119", "v120", "v121", "v122", "v123", "v124", "v125", "v126", "v127", "v128", "v129", "v130", "v131", "v132", "v133", "v134", "v135", "v144", "v145", "v146", "v147", "v148", "v149", "v150", "v151", "v152", "v153", "v154", "v155", "v156", "v157", "v158", "v159", "v160", "v161", "v162", "v163", "v164", "v165", "v166", "v167", "v168", "v169", "v170", "v171", "v172", "v173", "v174", "v175");
}


struct Acc { f32x4 q[8], k[8], v[8], hk, hv; };
struct Frag { s16x4 wq0, wq1, wk0, wk1, wv0, wv1; bf16x8 x[8], xh; };
__device__ __forceinline__ bf16x8 cat(s16x4 a, s16x4 b) { return (bf16x8){a[0], a[1], a[2], a[3], b[0], b[1], b[2], b[3]}; }
#define MEET(N) asm volatile("s_waitcnt vmcnt(" #N ")\n\ts_barrier" ::: "memory")

__device__ __forceinline__ void feed_ring(bool A, const char* x, const char* W, unsigned xvo, unsigned wvo, int rot, int rb, unsigned lds0, int lane, int hh) {
    unsigned hvo0 = 0, hvo1 = 0;
    if (A) {
        const int hr = 2 * hh + (lane >> 5), h0 = rb * 128 - 16 + 2 * hr;
        hvo0 = ((h0 < 0 ? 0 : h0) * DM + ((((lane & 31) >> 2) ^ hh) * 8) + (lane & 3) * 2) * 2;
        hvo1 = ((h0 < 0 ? 0 : h0 + 1) * DM + ((((lane & 31) >> 2) ^ (4 + hh)) * 8) + (lane & 3) * 2) * 2;
        __builtin_amdgcn_s_sleep(28);
    }
    unsigned d_ = lds0 + hh * 1024;
#pragma unroll 1
    for (int t = 0; t < 8; ++t) {
        if (t >= 2) { if (A) MEET(12); else MEET(10); }
        const int s_ = (t + rot) & 7; const char* x_ = x + s_ * 128; const char* w_ = W + s_ * 196608;
        dma16<0>(w_, wvo, d_); dma16<0>(x_, xvo, d_ + OFF_X); dma16<0>(x_ + 32768, xvo, d_ + OFF_X + 4096); dma16<0>(x_ + 65536, xvo, d_ + OFF_X + 8192); dma16<0>(x_ + 98304, xvo, d_ + OFF_X + 12288);
        dma16<1024>(w_, wvo, d_ + 8192); dma16<2048>(w_, wvo, d_ + 16384); dma16<0>(w_ + 98304, wvo, d_ + 4096); dma16<1024>(w_ + 98304, wvo, d_ + 12288); dma16<2048>(w_ + 98304, wvo, d_ + 20480);
        if (A) { dma4<0>(x_, hvo0, d_ - hh * 768 + OFF_H); dma4<0>(x_, hvo1, d_ - hh * 768 + OFF_H + 1024); }
        d_ += (t == 2 || t == 5) ? -2 * STAGE : STAGE;
    }
    if (A) MEET(12); else MEET(10);
    MEET(0); MEET(0);
}
template <bool A>
__device__ __forceinline__ void project(Acc& a, Frag& f0, Frag& f1, lds_ptr lds, int lane, int hh) {
    const int g = lane >> 4, r16 = lane & 15;
    const int xo = OFF_X + r16 * 128 + ((g ^ (r16 >> 1)) * 16), ho = OFF_H + r16 * 128 + ((g ^ (r16 >> 1)) * 16);
    const int wo = (8 * g + (r16 >> 2)) * 128 + ((hh ^ (((r16 >> 3) & 1) | ((g & 1) << 1))) * 32) + (lane & 3) * 8;
#define HALF(c, n, so, ks, rd_) do { lds_ptr sw = lds + (so) + wo + (ks) * 4096, sx = lds + (so) + (xo ^ ((ks) * 64)); \
        const bf16x8 wq_ = cat(c.wq0, c.wq1), wk_ = cat(c.wk0, c.wk1), wv_ = cat(c.wv0, c.wv1); \
        _Pragma("unroll") for (int i = 0; i < 8; ++i) { a.q[i] = MFMA16(wq_, c.x[i], a.q[i]); \
            if (rd_) { if (i == 0) n.wq0 = trread(sw); if (i == 1) n.wq1 = trread(sw + 512); if (i >= 2 && !(i & 1)) n.x[i / 2 - 1] = ldsfrag(sx + (i / 2 - 1) * 2048); } SBAR(); } \
        _Pragma("unroll") for (int i = 0; i < 8; ++i) { a.k[i] = MFMA16(wk_, c.x[i], a.k[i]); \
            if (rd_) { if (!(i & 1)) n.x[3 + i / 2] = ldsfrag(sx + (3 + i / 2) * 2048); if (i == 7) n.x[7] = ldsfrag(sx + 7 * 2048); } SBAR(); } \
        _Pragma("unroll") for (int i = 0; i < 8; ++i) { a.v[i] = MFMA16(c.x[i], wv_, a.v[i]); \
            if (rd_) { if (i == 0) n.wk0 = trread(sw + 8192); if (i == 1) n.wk1 = trread(sw + 8704); if (i == 2) n.wv0 = trread(sw + 16384); if (i == 3) n.wv1 = trread(sw + 16896); \
                       if (i == 4 && A) n.xh = ldsfrag(lds + (so) + (ho ^ ((ks) * 64))); } SBAR(); } \
        if (A) { a.hk = MFMA16(wk_, c.xh, a.hk); a.hv = MFMA16(c.xh, wv_, a.hv); SBAR(); } } while (0)
#define FIRST() do { lds_ptr sw = lds + wo, sx = lds + xo; f0.wq0 = trread(sw); f0.wq1 = trread(sw + 512); \
        _Pragma("unroll") for (int i = 0; i < 8; ++i) f0.x[i] = ldsfrag(sx + i * 2048); \
        f0.wk0 = trread(sw + 8192); f0.wk1 = trread(sw + 8704); f0.wv0 = trread(sw + 16384); f0.wv1 = trread(sw + 16896); if (A) f0.xh = ldsfrag(lds + ho); } while (0)
#define PINACC() asm volatile("" : "+v"(a.q[0]), "+v"(a.q[1]), "+v"(a.q[2]), "+v"(a.q[3]), "+v"(a.q[4]), "+v"(a.q[5]), "+v"(a.q[6]), "+v"(a.q[7]), "+v"(a.k[0]), "+v"(a.k[1]), "+v"(a.k[2]), "+v"(a.k[3]), \
        "+v"(a.k[4]), "+v"(a.k[5]), "+v"(a.k[6]), "+v"(a.k[7]), "+v"(a.v[0]), "+v"(a.v[1]), "+v"(a.v[2]), "+v"(a.v[3]), "+v"(a.v[4]), "+v"(a.v[5]), "+v"(a.v[6]), "+v"(a.v[7]))
    int so = 0;
    PINACC(); if (A) asm volatile("" : "+v"(a.hk), "+v"(a.hv));
    LDS_BAR();
    FIRST();
#pragma unroll 1
    for (int t = 0; t < 7; ++t) {
        const int sn = so == 2 * STAGE ? 0 : so + STAGE;
        HALF(f0, f1, so, 1, true); LDS_BAR(); HALF(f1, f0, sn, 0, true);
        so = sn;
    }
    if (A) { HALF(f0, f1, STAGE, 1, true); LDS_BAR(); HALF(f1, f0, 0, 0, false); }
    else {
        lds_ptr sw = lds + STAGE + wo + 4096, sx = lds + STAGE + (xo ^ 64);
        const bf16x8 wk_ = cat(f0.wk0, f0.wk1);
        f1.wk0 = trread(sw + 8192); f1.wk1 = trread(sw + 8704);
#pragma unroll
        for (int i = 0; i < 8; ++i) { a.k[i] = MFMA16(wk_, f0.x[i], a.k[i]); f1.x[i] = ldsfrag(sx + i * 2048);
            if (i == 4) f1.wv0 = trread(sw + 16384); if (i == 5) f1.wv1 = trread(sw + 16896); if (i == 6) f1.wq0 = trread(sw); if (i == 7) f1.wq1 = trread(sw + 512); SBAR(); }
        const bf16x8 wl_ = cat(f1.wk0, f1.wk1);
#pragma unroll
        for (int i = 0; i < 8; ++i) a.k[i] = MFMA16(wl_, f1.x[i], a.k[i]);
        LDS_BAR();
    }
#undef HALF
#undef FIRST
#undef PINACC
}

__global__ __launch_bounds__(512, 2) void fused_mha(const u16* xa, const u16* wa, const u16* woa,
                                                    const u16* xb, const u16* wb, const u16* wob,
                                                    u16* out, char* ws) {
    extern __shared__ __attribute__((aligned(16))) char ldsg[];
    lds_ptr lds = (lds_ptr)ldsg; const unsigned lds0 = (unsigned)(uintptr_t)ldsg;
    const int tid = threadIdx.x, lane = tid & 63; const int wid = rfl(tid >> 6);
    const int g = lane >> 4, r16 = lane & 15, hh = wid & 3;
    const int xcd = blockIdx.x & 7, idx = blockIdx.x >> 3;
    const bool roleA = idx & 1;
    const int rb = (xcd >> 1) * 4 + ((idx >> 1) & 3), cg = (xcd & 1) * 4 + (idx >> 3);
    unsigned xvo, wvo;
    {
        const int rho = hh * 8 + (lane >> 3), xrow = roleA ? 2 * (rho & 15) + (rho >> 4) : rho;
        xvo = ((rb * 128 + xrow) * DM + (((lane & 7) ^ ((4 * hh + (lane >> 4)) & 7)) * 8)) * 2;
        wvo = (rho * 1536 + cg * 64 + (((lane & 7) ^ ((((lane >> 4) & 1) | ((hh & 1) << 1)) << 1)) * 8)) * 2;
        asm volatile("" : "+v"(xvo), "+v"(wvo));
        SBAR();
        asm volatile("" : "+s"(xa), "+s"(wa), "+s"(woa), "+s"(xb), "+s"(wb), "+s"(wob), "+s"(out), "+s"(ws));
    }
    if (__builtin_expect(wid < 4, 1)) feed_ring(roleA, (const char*)(roleA ? xa : xb), (const char*)(roleA ? wa : wb), xvo, wvo, ((rb + cg) & 3) * 2, rb, lds0, lane, hh);
    const __amdgpu_buffer_rsrc_t wsr = __builtin_amdgcn_make_buffer_rsrc(ws, 0, WS_END, 0x00020000);
    const int head = xcd * 4 + (idx & 3), qb = idx >> 2;
    const unsigned kvo = (head * 64 + (wid >> 2) * 32 + hh) * 1024 + lane * 16, kvd = lds0 + ((wid >> 2) * 32 + hh) * 1024;
    unsigned ctl = lds0 + L_CTL, one = 1; asm volatile("" : "+v"(ctl), "+v"(one));
    const int orb = xcd * 4 + (idx & 3), ocb = idx >> 2;
    const int rg = hh, kh = wid >> 2, qt = qb * 8 + hh * 2;
    unsigned ovo, wvo2, obo, oo2, ka, va, vinc; lds_ptr cb, cb2, qp, rx, ry, wb_[4]; const char* fA; const char* wpf;
    auto addresses = [&](bool feeder) {
        const int wro = (16 * (g >> 1) + 4 * (g & 1) + (r16 >> 2)) * 128 + (lane & 3) * 8, phi = ((g & 1) << 1) | (r16 >> 3);
        ovo = ((orb * 2 + (rg >> 1)) * 32 + (g >> 1)) * 1024 + (g & 1) * 512 + ((rg & 1) * 16 + r16) * 16;
        if (feeder) { wvo2 = ((rg * 8 + (lane >> 3)) * DM + ocb * 64 + (((lane & 7) ^ (((lane >> 4) & 3) << 1)) * 8)) * 2;
                      fA = ws + FL_A + ((orb >> 1) * 8 + (lane & 7)) * 64; asm volatile("" : "+v"(wvo2), "+v"(fA)); }
        else { obo = WS_OB + (qt * 32 + head) * 1024 + lane * 16; asm volatile("" : "+v"(obo)); }
        cb = lds + L_COMB + rg * 4608 + lane * 16; cb2 = lds + L_COMB + rg * 4608 + 4096 + lane * 8; qp = lds + L_Q + hh * 2048 + lane * 16;
#pragma unroll
        for (int nb = 0; nb < 4; ++nb) { wb_[nb] = lds + kh * L_V + wro + ((nb ^ (2 * kh) ^ phi) * 32); asm volatile("" : "+v"(wb_[nb])); }
        oo2 = ((orb * 64 + rg * 16 + r16) * 1024 + ocb * 64 + kh * 32 + (g & 1) * 16 + (g >> 1) * 8) * 2;
        rx = lds + 32768 + wid * 2048 + lane * 16; ry = lds + 32768 + (wid ^ 4) * 2048 + lane * 16;
        ka = lds0 + kh * 32768 + lane * 16;
        va = (lane & 16) ? lds0 + L_ONE + (r16 == 0 ? 16 : 0) : lds0 + L_V + kh * 32768 + (lane >> 5) * 256 + r16 * 16;
        vinc = (lane & 16) ? 0u : 4096u;
        wpf = (const char*)(kh ? wob : woa) + (idx * 128 + hh * 32 + (lane & 31)) * 128;
        asm volatile("" : "+v"(ovo), "+v"(cb), "+v"(cb2), "+v"(qp), "+v"(ka), "+v"(va), "+v"(vinc), "+v"(wpf), "+v"(oo2), "+v"(rx), "+v"(ry));
    };
    u32x4 oal[4] = {}; unsigned oo = 0;
    u64 ep;
    auto tag = [&]() { const u32x2 t = *(LDSP(const u32x2))(lds + L_CTL + 48); return ((u64)rfl(t[1]) << 32) | rfl(t[0]); };
    if (wid < 4) {
        ep = tag();
        addresses(true);
        if (wid == 0) {
            if (!roleA) lds_wait<20, 4>(ctl); else __builtin_amdgcn_s_sleep(10);
            spin_until(ws + FL_B1 + (xcd * 16 + r16) * 64, ep);
            lds_arrive<32>(ctl, one);
        } else lds_wait<32, 1>(ctl);
#define KV(c) do { dma16_dev<0>(ws + WS_K + (c) * 4096, kvo, kvd + (c) * 4096); dma16_dev<0>(ws + WS_K + 32768 + (c) * 4096, kvo, kvd + 32768 + (c) * 4096); \
        dma16_dev<0>(ws + WS_V + (c) * 4096, kvo, kvd + L_V + (c) * 4096); dma16_dev<0>(ws + WS_V + 32768 + (c) * 4096, kvo, kvd + L_V + 32768 + (c) * 4096); } while (0)
        { const unsigned qvo = (head * 64 + qb * 8 + hh * 2) * 1024 + lane * 16;
          dma16_dev<0>(ws + WS_Q, qvo, lds0 + L_Q + hh * 2048); dma16_dev<1024>(ws + WS_Q, qvo, lds0 + L_Q + hh * 2048 + 1024); }
        KV(0); KV(1);
        MEET(4);
    } else {
        if (tid == 256) {
            const u64 t = __hip_atomic_load((gu64*)(ws + WS_FL), RLX_AGENT) + 1;
            *(LDSP(u64))(lds + L_CTL + 48) = t;
            if (roleA) __hip_atomic_store((gu64*)(ws + FL_H + (rb * 8 + cg) * 64), t, RLX_AGENT);
        }
        if (wid == 6 && lane < 12) *(LDSP(unsigned))(lds + L_CTL + lane * 4) = 0;
        if (wid == 5 && lane < 20) { const unsigned c_ = (lane & 1) ? 0x3f803f80u : 0u; *(LDSP(u32x4))(lds + L_ONE + (lane >> 1) * 512 + (lane & 1) * 16) = (u32x4){c_, c_, c_, c_}; }
        addresses(false);
        if (!roleA) {
            Acc a = {}; Frag f0, f1;
            const unsigned fo = ((cg * 4 + hh) * 64 + rb * 4) * 1024;
            unsigned qo = fo + ((lane >> 4) & 1) * 512 + (r16 + 16 * (lane >> 5)) * 16, vo = fo + lane * 16;
            asm volatile("" : "+v"(qo), "+v"(vo));
            project<false>(a, f0, f1, lds, lane, hh);
            ep = tag();
            { const bf16x8 w0 = cat(f0.wv0, f0.wv1), w1 = cat(f1.wv0, f1.wv1);
#pragma unroll
              for (int j = 0; j < 4; ++j) {
                  a.v[2 * j] = MFMA16(f0.x[2 * j], w0, a.v[2 * j]); a.v[2 * j + 1] = MFMA16(f0.x[2 * j + 1], w0, a.v[2 * j + 1]);
                  a.v[2 * j] = MFMA16(f1.x[2 * j], w1, a.v[2 * j]); a.v[2 * j + 1] = MFMA16(f1.x[2 * j + 1], w1, a.v[2 * j + 1]);
                  __builtin_amdgcn_raw_buffer_store_b128(pack_pair(a.k[2 * j], a.k[2 * j + 1], 1.0f, 1.0f), wsr, WS_K + qo + j * 1024, 0, 16); SBAR(); } }
            { const bf16x8 w0 = cat(f0.wq0, f0.wq1), w1 = cat(f1.wq0, f1.wq1);
#pragma unroll
              for (int j = 0; j < 4; ++j) {
                  a.q[2 * j] = MFMA16(w0, f0.x[2 * j], a.q[2 * j]); a.q[2 * j + 1] = MFMA16(w0, f0.x[2 * j + 1], a.q[2 * j + 1]);
                  a.q[2 * j] = MFMA16(w1, f1.x[2 * j], a.q[2 * j]); a.q[2 * j + 1] = MFMA16(w1, f1.x[2 * j + 1], a.q[2 * j + 1]);
                  __builtin_amdgcn_raw_buffer_store_b128(pack_pair(a.v[2 * j], a.v[2 * j + 1], 1.0f, 1.0f), wsr, WS_V + vo + j * 1024, 0, 16); SBAR(); } }
#pragma unroll
            for (int j = 0; j < 4; ++j) __builtin_amdgcn_raw_buffer_store_b128(pack_pair(a.q[2 * j], a.q[2 * j + 1], QSCALE, QSCALE), wsr, WS_Q + qo + j * 1024, 0, 16);
            asm volatile("s_waitcnt vmcnt(0)" ::: "memory");
            if (lds_arrive_rtn<20>(ctl, one) == 3 && lane == 0) __hip_atomic_store((gu64*)(ws + FL_B1 + (cg * 16 + rb) * 64), ep, RLX_AGENT);
            if (blockIdx.x == 0 && wid == 5) {
                const u64 t0 = __builtin_amdgcn_s_memrealtime();
                for (;;) {
                    const gu64* f_ = (const gu64*)(ws + FL_B1 + lane * 64);
                    const u64 a0 = __hip_atomic_load(f_, RLX_AGENT), a1 = __hip_atomic_load(f_ + 512, RLX_AGENT), a2 = __hip_atomic_load(f_ + 1024, RLX_AGENT), a3 = __hip_atomic_load(f_ + 1536, RLX_AGENT);
                    if (__all(a0 == ep && a1 == ep && a2 == ep && a3 == ep) || __builtin_amdgcn_s_memrealtime() - t0 > 5000000ull) break;
                    __builtin_amdgcn_s_sleep(4);
                }
                if (lane == 0) __hip_atomic_store((gu64*)(ws + WS_FL), ep, RLX_AGENT);
            }
        } else {
            lds_ptr mp = lds + L_MASK + lane * 16;
            if (hh == 0) {
                f32x4 m0, m1, m2, m3;
#pragma unroll
                for (int r = 0; r < 4; ++r) { const int rho = 4 * g + r;
                    m0[r] = (rho <= r16 && rho + 7 >= r16) ? 0.f : -INFINITY; m1[r] = rho >= r16 + 9 ? 0.f : -INFINITY;
                    m2[r] = (rb != 0 && rho < 8 && rho >= r16 + 1) ? 0.f : -INFINITY; m3[r] = (rb != 0 && rho >= r16 + 9) ? 0.f : -INFINITY; }
                *(LDSP(f32x4))mp = m0; *(LDSP(f32x4))(mp + 1024) = m1; *(LDSP(f32x4))(mp + 2048) = m2; *(LDSP(f32x4))(mp + 3072) = m3;
            }
            Acc a = {}; Frag f0, f1;
            project<true>(a, f0, f1, lds, lane, hh);
            ep = tag();
            const f32x4 mOwn = *(LDSP(const f32x4))mp, mPrev = *(LDSP(const f32x4))(mp + 1024), mH[2] = {*(LDSP(const f32x4))(mp + 2048), *(LDSP(const f32x4))(mp + 3072)};
            s16x4 kf[8], vf[8]; const s16x4 hk = cvt4(a.hk, 1.0f), hv = cvt4(a.hv, 1.0f);
            f32x4 s0[8], s1[8]; float m[8];
#pragma unroll
            for (int b = 0; b < 8; ++b) { kf[b] = cvt4(a.k[b], 1.0f); const s16x4 qf = cvt4(a.q[b], QSCALE);
                s0[b] = MFMA16K16(b < 2 ? hk : kf[b < 2 ? 0 : b - 2], qf, b < 2 ? mH[b & 1] : mPrev); s1[b] = MFMA16K16(kf[b], qf, mOwn); }
#pragma unroll
            for (int b = 0; b < 8; ++b) vf[b] = cvt4(a.v[b], 1.0f);
            SBAR();
#pragma unroll
            for (int b = 0; b < 8; ++b) m[b] = max3(max3(s0[b][0], s0[b][1], s0[b][2]), max3(s0[b][3], s1[b][0], s1[b][1]), max2(s1[b][2], s1[b][3]));
#pragma unroll
            for (int b = 0; b < 8; b += 2) {
                auto r0 = __builtin_amdgcn_permlane32_swap(__float_as_uint(m[b]), __float_as_uint(m[b + 1]), false, false);
                unsigned t = __float_as_uint(max2(__uint_as_float(r0[0]), __uint_as_float(r0[1])));
                auto r1 = __builtin_amdgcn_permlane16_swap(t, t, false, false);
                t = __float_as_uint(max2(__uint_as_float(r1[0]), __uint_as_float(r1[1])));
                auto r2 = __builtin_amdgcn_permlane32_swap(t, t, false, false);
                m[b] = __uint_as_float(r2[0]); m[b + 1] = __uint_as_float(r2[1]); }
            oo = WS_OA + (rb * 4 * 32 + cg * 4 + hh) * 1024 + ((lane >> 4) & 1) * 512 + (2 * r16 + (lane >> 5)) * 16;
            const s16x4 on4 = {0x3f80, 0x3f80, 0x3f80, 0x3f80};
#pragma unroll
            for (int b = 0; b < 8; b += 2) {
                f32x4 o[2]; float rl[2];
#pragma unroll
                for (int c = b; c < b + 2; ++c) {
                    const f32x2 nm = {-m[c], -m[c]};
                    f32x2 t0 = (f32x2){s0[c][0], s0[c][1]} + nm, t1 = (f32x2){s0[c][2], s0[c][3]} + nm, t2 = (f32x2){s1[c][0], s1[c][1]} + nm, t3 = (f32x2){s1[c][2], s1[c][3]} + nm;
                    asm volatile("" : "+v"(t0), "+v"(t1), "+v"(t2), "+v"(t3));
                    s0[c] = (f32x4){t0[0], t0[1], t1[0], t1[1]}; s1[c] = (f32x4){t2[0], t2[1], t3[0], t3[1]};
#pragma unroll
                    for (int r = 0; r < 4; ++r) { s0[c][r] = __builtin_amdgcn_exp2f(s0[c][r]); s1[c][r] = __builtin_amdgcn_exp2f(s1[c][r]); }
                    const s16x4 p0 = cvt4(s0[c], 1.0f), p1 = cvt4(s1[c], 1.0f);
                    o[c - b] = MFMA16K16(vf[c], p1, MFMA16K16(c < 2 ? hv : vf[c < 2 ? 0 : c - 2], p0, (f32x4){}));
                    rl[c - b] = __builtin_amdgcn_rcpf(MFMA16K16(on4, p1, MFMA16K16(on4, p0, (f32x4){}))[0]); }
                oal[b >> 1] = pack_pair(o[0], o[1], rl[0], rl[1]);
            }
        }
        LDS_BAR();
    }
    const __amdgpu_buffer_rsrc_t outr = __builtin_amdgcn_make_buffer_rsrc(out, 0, 2048 * 1024 * 2, 0x00020000);
    const bf16x8 qa = ldsfrag(qp), qc = ldsfrag(qp + 1024);
    f32x16 oa, ob;
    {
        const bf16x8 k0 = ldsfrag(lds + lane * 16);
        const float ma = halfmax(rowmax16(MFMA32(k0, qa, (f32x16){}))), mb = halfmax(rowmax16(MFMA32(k0, qc, (f32x16){})));
        if (roleA && wid >= 4) {
#pragma unroll
            for (int j = 0; j < 4; ++j) __builtin_amdgcn_raw_buffer_store_b128(oal[j], wsr, oo + j * 32768, 0, 16);
        }
        __builtin_amdgcn_s_setprio(1);
        attn_keyloop(oa, ob, qa, qc, ma, mb, ka, va, vinc, kvo + 4096, ws + WS_K, ws + WS_V, kvd, rfl(roleA && wid == 0),
                     ws + FL_A + (rb * 8 + cg) * 64, ep,
                     wpf);
    }
    if (wid < 4) {
        __builtin_amdgcn_s_setprio(0);
        u64 pv; asm volatile("global_load_dwordx2 %0, %1, off sc1" : "=v"(pv) : "v"(fA) : "memory");
        *(LDSP(f32x4))cb = (f32x4){oa[0], oa[1], oa[2], oa[3]}; *(LDSP(f32x4))(cb + 1024) = (f32x4){oa[4], oa[5], oa[6], oa[7]};
        *(LDSP(f32x4))(cb + 2048) = (f32x4){ob[0], ob[1], ob[2], ob[3]}; *(LDSP(f32x4))(cb + 3072) = (f32x4){ob[4], ob[5], ob[6], ob[7]};
        *(LDSP(f32x2))cb2 = (f32x2){oa[8], ob[8]};
        asm volatile("s_waitcnt lgkmcnt(0)" ::: "memory");
        lds_arrive<0>(ctl, one);
        lds_wait<0, 4>(ctl);
#pragma unroll
        for (int i = 0; i < 16; ++i) dma16<0>((const char*)woa + i * 32768, wvo2, lds0 + (i >> 3) * L_V + (i & 7) * 4096 + rg * 1024);
        asm volatile("s_waitcnt vmcnt(16)" : "+v"(pv) :: "memory");
        if (!__all(pv == ep)) spin_until(fA, ep);
        bf16x8 of[16];
#pragma unroll
        for (int s = 0; s < 8; ++s) { ld16_dev<0>(of[2 * s], ws + WS_OA + s * 4096, ovo); ld16_dev<2048>(of[2 * s + 1], ws + WS_OA + s * 4096, ovo); }
        asm volatile("s_waitcnt vmcnt(16)" ::: "memory");
        lds_arrive<4>(ctl, one); lds_wait<4, 4>(ctl);
        bf16x8 wp[8][4];
        f32x4 acc[4] = {};
#pragma unroll
        for (int h = 0; h < 2; ++h) {
            lds_ptr wh[4];
#pragma unroll
            for (int nb = 0; nb < 4; ++nb) { wh[nb] = wb_[nb] + h * L_V; asm volatile("" : "+v"(wh[nb])); }
#pragma unroll
            for (int s = 0; s < 8; ++s)
#pragma unroll
                for (int nb = 0; nb < 4; ++nb) wp[s][nb] = trfrag(wh[nb] + s * 4096, 1024);
            asm volatile("s_waitcnt lgkmcnt(0)" ::: "memory");
            lds_arrive<24>(ctl, one); if (h == 0) lds_wait<24, 4>(ctl); else lds_wait<24, 8>(ctl);
#pragma unroll
            for (int i = 0; i < 8; ++i) dma16<0>((const char*)wob + (8 * h + i) * 32768, wvo2, lds0 + h * L_V + i * 4096 + rg * 1024);
            if (h == 0) {
                asm volatile("s_waitcnt vmcnt(8)" : "+v"(of[0]), "+v"(of[1]), "+v"(of[2]), "+v"(of[3]), "+v"(of[4]), "+v"(of[5]), "+v"(of[6]), "+v"(of[7]),
                             "+v"(of[8]), "+v"(of[9]), "+v"(of[10]), "+v"(of[11]), "+v"(of[12]), "+v"(of[13]), "+v"(of[14]), "+v"(of[15]) :: "memory");
#pragma unroll
                for (int s = 0; s < 8; ++s)
#pragma unroll
                    for (int nb = 0; nb < 4; ++nb) acc[nb] = MFMA16(wp[s][nb], of[s], acc[nb]);
                SBAR();
            }
        }
        asm volatile("s_waitcnt vmcnt(0)" ::: "memory");
        lds_arrive<12>(ctl, one);
        lds_wait<8, 4>(ctl);
        SBAR();
#pragma unroll
        for (int s = 0; s < 8; ++s)
#pragma unroll
            for (int nb = 0; nb < 4; ++nb) acc[nb] = MFMA16(wp[s][nb], of[8 + s], acc[nb]);
        __builtin_amdgcn_raw_buffer_store_b128(pack_cols(acc[0], acc[1]), outr, oo2, 0, 16);
        __builtin_amdgcn_raw_buffer_store_b128(pack_cols(acc[2], acc[3]), outr, oo2 + 64, 0, 16);
    } else {
        lds_wait<0, 4>(ctl);
        const f32x4 p0 = *(LDSP(const f32x4))cb, p1 = *(LDSP(const f32x4))(cb + 1024), p2 = *(LDSP(const f32x4))(cb + 2048), p3 = *(LDSP(const f32x4))(cb + 3072);
        const f32x2 pl = *(LDSP(const f32x2))cb2;
#pragma unroll
        for (int i = 0; i < 4; ++i) { oa[i] += p0[i]; oa[4 + i] += p1[i]; ob[i] += p2[i]; ob[4 + i] += p3[i]; }
        oa[8] += pl[0]; ob[8] += pl[1];
        f32x2 z = {0.f, 0.f};
#pragma unroll
        for (int i = 0; i < 8; i += 2) { z = __builtin_elementwise_fma((f32x2){oa[i], oa[i + 1]}, (f32x2){0.f, 0.f}, z); z = __builtin_elementwise_fma((f32x2){ob[i], ob[i + 1]}, (f32x2){0.f, 0.f}, z); }
        if (__builtin_expect(!__all(oa[8] <= 1.2676506e30f && ob[8] <= 1.2676506e30f && z[0] + z[1] == 0.0f), 0)) {
            const unsigned kg = WS_K + head * 65536 + lane * 16, vg = WS_V + head * 65536 + (lane >> 5) * 256 + r16 * 16;
            float ma = -INFINITY, mb = -INFINITY;
#pragma unroll 1
            for (int t = 0; t < 64; ++t) { const bf16x8 kf = __builtin_bit_cast(bf16x8, __builtin_amdgcn_raw_buffer_load_b128(wsr, kg + t * 1024, 0, 16));
                ma = max2(ma, rowmax16(MFMA32(kf, qa, (f32x16){}))); mb = max2(mb, rowmax16(MFMA32(kf, qc, (f32x16){}))); }
            ma = halfmax(ma); mb = halfmax(mb);
            oa = (f32x16){}; ob = (f32x16){};
            const unsigned one = (lane & 31) == 16 ? 0x3f803f80u : 0u;
#pragma unroll 1
            for (int t = 0; t < 64; ++t) {
                const bf16x8 kf = __builtin_bit_cast(bf16x8, __builtin_amdgcn_raw_buffer_load_b128(wsr, kg + t * 1024, 0, 16));
                u32x4 v0 = __builtin_amdgcn_raw_buffer_load_b128(wsr, vg + t * 1024, 0, 16), v1 = __builtin_amdgcn_raw_buffer_load_b128(wsr, vg + t * 1024 + 512, 0, 16);
                if (lane & 16) { v0 = (u32x4){one, one, one, one}; v1 = v0; }
                f32x16 sa = MFMA32(kf, qa, splat16(-ma)), sb = MFMA32(kf, qc, splat16(-mb));
#pragma unroll
                for (int i = 0; i < 16; ++i) { sa[i] = __builtin_amdgcn_exp2f(sa[i]); sb[i] = __builtin_amdgcn_exp2f(sb[i]); }
#define PK8(s, o_) __builtin_bit_cast(bf16x8, (u32x4){cvtpk(s[o_], s[o_ + 1]), cvtpk(s[o_ + 2], s[o_ + 3]), cvtpk(s[o_ + 4], s[o_ + 5]), cvtpk(s[o_ + 6], s[o_ + 7])})
                oa = MFMA32(__builtin_bit_cast(bf16x8, v0), PK8(sa, 0), oa); oa = MFMA32(__builtin_bit_cast(bf16x8, v1), PK8(sa, 8), oa);
                ob = MFMA32(__builtin_bit_cast(bf16x8, v0), PK8(sb, 0), ob); ob = MFMA32(__builtin_bit_cast(bf16x8, v1), PK8(sb, 8), ob);
            }
        }
        auto fin = [&](const f32x16& o, int which) {
            auto rr = __builtin_amdgcn_permlane32_swap(__float_as_uint(o[8]), __float_as_uint(o[8]), false, false);
            const float rl = __builtin_amdgcn_rcpf(__uint_as_float(rr[0]));
            u32x4 ov = {cvtpk(o[0] * rl, o[1] * rl), cvtpk(o[2] * rl, o[3] * rl), cvtpk(o[4] * rl, o[5] * rl), cvtpk(o[6] * rl, o[7] * rl)};
            __builtin_amdgcn_raw_buffer_store_b128(ov, wsr, obo + which * 32768, 0, 16); };
        fin(oa, 0); fin(ob, 1);
        lds_arrive<8>(ctl, one);
        asm volatile("s_waitcnt vmcnt(0)" ::: "memory");
        if (lds_arrive_rtn<16>(ctl, one) == 3 && lane == 0) __hip_atomic_store((gu64*)(ws + FL_B2 + (qb * 32 + head) * 64), ep, RLX_AGENT);
        __builtin_amdgcn_s_setprio(0);
    }
    lds_wait<12, 4>(ctl);
    bf16x8 wf[8][4];
#pragma unroll
    for (int s = 0; s < 8; ++s)
#pragma unroll
        for (int nb = 0; nb < 4; ++nb) wf[s][nb] = trfrag(wb_[nb] + s * 4096, 1024);
    if (wid == 4) spin_until<1>(ws + FL_B2 + ((orb >> 2) * 32 + (lane & 31)) * 64, ep);
    LDS_BAR();
    {
        bf16x8 of[8];
        const char* ob_ = ws + WS_OB + kh * 16384;
#pragma unroll
        for (int s = 0; s < 4; ++s) { ld16_dev<0>(of[2 * s], ob_ + s * 4096, ovo); ld16_dev<2048>(of[2 * s + 1], ob_ + s * 4096, ovo); }
        f32x4 acc[4] = {};
#define OSTEP(s, N) do { asm volatile("s_waitcnt vmcnt(" #N ")" : "+v"(of[s]) :: "memory"); \
        acc[0] = MFMA16(wf[s][0], of[s], acc[0]); acc[1] = MFMA16(wf[s][1], of[s], acc[1]); acc[2] = MFMA16(wf[s][2], of[s], acc[2]); acc[3] = MFMA16(wf[s][3], of[s], acc[3]); SBAR(); } while (0)
        OSTEP(0, 7); OSTEP(1, 6); OSTEP(2, 5); OSTEP(3, 4); OSTEP(4, 3); OSTEP(5, 2); OSTEP(6, 1); OSTEP(7, 0);
        *(LDSP(f32x4))rx = acc[2]; *(LDSP(f32x4))(rx + 1024) = acc[3];
        LDS_BAR();
        const f32x4 b0 = *(LDSP(const f32x4))ry, b1 = *(LDSP(const f32x4))(ry + 1024);
        __builtin_amdgcn_raw_buffer_store_b128(pack_cols(acc[0] + b0, acc[1] + b1), outr, oo2 + 1024, 0, 16);
    }
}

extern "C" void kernel_launch(void* const* d_in, const int* in_sizes, int n_in, void* d_out, int out_size, void* d_ws, size_t ws_size, hipStream_t stream) {
    (void)hipFuncSetAttribute((const void*)fused_mha, hipFuncAttributeMaxDynamicSharedMemorySize, LDS_BYTES);
    fused_mha<<<256, 512, LDS_BYTES, stream>>>((const u16*)d_in[0], (const u16*)d_in[1], (const u16*)d_in[2], (const u16*)d_in[3], (const u16*)d_in[4], (const u16*)d_in[5],
                                               (u16*)d_out, (char*)d_ws);
}
```
